```python
import torch

def _softmax(x, axis=-1):
    return torch.softmax(x, dim=axis)
def _gelu(x):
    return torch.nn.functional.gelu(x, approximate="tanh")
def _silu(x):
    return torch.nn.functional.silu(x)
def _sigmoid(x):
    return torch.sigmoid(x)
def _layernorm(x, axis=-1, eps=1e-5):
    m = x.mean(dim=axis, keepdim=True)
    v = x.var(dim=axis, keepdim=True, unbiased=False)
    return (x - m) / torch.sqrt(v + eps)
def _scatter_add(tgt, idx, src, axis=0):
    return tgt.clone().index_add_(axis, idx, src)
def _segment_sum(src, seg, n):
    out = torch.zeros((n,) + src.shape[1:], dtype=src.dtype)
    return out.index_add_(0, seg, src)
def _pad(x, pw):
    flat = tuple(v for pair in reversed(pw) for v in pair)
    return torch.nn.functional.pad(x, flat)

kernel_name = 'standalone__global_local_windowed_global_tokens__prompt'

def setup_inputs():

    _w = (lambda a: (lambda t: t.to(torch.bfloat16) if t.is_floating_point() else t)(torch.as_tensor(a)))
    B, H, S, D = 1, 32, 2048, 128
    return {
        "q": _w((lambda *s: torch.randn(*s, dtype=torch.bfloat16) * 0.8)(B, H, S, D)),
        "k": _w((lambda *s: torch.randn(*s, dtype=torch.bfloat16) * 0.8)(B, H, S, D)),
        "v": _w((lambda *s: torch.randn(*s, dtype=torch.bfloat16) * 0.05)(B, H, S, D)),
        "window": 32,
        "n_global": 2,
    }

def reference(q, k, v, window, n_global):
    B, H, S, D = q.shape
    scale = D ** -0.5
    scores = torch.einsum("bhqd,bhkd->bhqk", q, k) * scale
    idx = torch.arange(S)
    dist = idx[:, None] - idx[None, :]
    local = (dist <= window) & (dist >= -window)
    is_global_q = (idx < n_global)[:, None]
    is_global_k = (idx < n_global)[None, :]
    mask = local | is_global_q | is_global_k
    scores = torch.where(mask[None, None], scores, -1e9)
    p = _softmax(scores, axis=-1)
    return torch.einsum("bhqk,bhkd->bhqd", p, v)
```

```cpp
#include <hip/hip_runtime.h>
#include <hip/hip_bf16.h>

#define POL_KV ""
#define POL_Q ""
#define AUX_ST 0

namespace {

constexpr int NH = 32, SEQ = 2048, HD = 128;
constexpr int ROWB = HD * 2;
constexpr int HEADB = SEQ * ROWB;
constexpr int CH = 256;
constexpr int NCH = SEQ / CH;
constexpr int TILEB = 16 * ROWB;
constexpr int VB = 0;
constexpr int KB = 18 * TILEB;
constexpr int VGB = 38 * TILEB;
constexpr int LDSB = 39 * TILEB;
constexpr int WS_LSE = 16384, WS_O = 65536;

typedef short bf16x8 __attribute__((ext_vector_type(8)));
typedef __bf16 bfx8 __attribute__((ext_vector_type(8)));
typedef short s16x4 __attribute__((ext_vector_type(4)));
typedef float f32x4 __attribute__((ext_vector_type(4)));
typedef unsigned u32x4 __attribute__((ext_vector_type(4)));
typedef unsigned u32x2 __attribute__((ext_vector_type(2)));
typedef unsigned long long u64;
typedef __attribute__((address_space(3))) char lds_char;
typedef __attribute__((address_space(1))) u64 gu64;
typedef __attribute__((address_space(1))) unsigned gu32;
#define DI __device__ __forceinline__
#define RLX_AGENT __ATOMIC_RELAXED, __HIP_MEMORY_SCOPE_AGENT

DI f32x4 mfma(u32x4 a, u32x4 b, f32x4 c) {
    return __builtin_amdgcn_mfma_f32_16x16x32_bf16(__builtin_bit_cast(bfx8, a), __builtin_bit_cast(bfx8, b), c, 0, 0, 0);
}
DI unsigned pk(float lo, float hi) {
    typedef __bf16 bfx2 __attribute__((ext_vector_type(2)));
    bfx2 r = {(__bf16)lo, (__bf16)hi};
    return __builtin_bit_cast(unsigned, r);
}
DI float max3(float a, float b, float c) { return fmaxf(fmaxf(a, b), c); }
DI float xmax(float m) {
    auto a = __builtin_amdgcn_permlane16_swap(__float_as_uint(m), __float_as_uint(m), false, false);
    m = fmaxf(__uint_as_float(a[0]), __uint_as_float(a[1]));
    auto b = __builtin_amdgcn_permlane32_swap(__float_as_uint(m), __float_as_uint(m), false, false);
    return fmaxf(__uint_as_float(b[0]), __uint_as_float(b[1]));
}
DI u32x2 trread(unsigned addr) {
    s16x4 r = __builtin_amdgcn_ds_read_tr16_b64_v4i16((__attribute__((address_space(3))) s16x4*)addr);
    return __builtin_bit_cast(u32x2, r);
}
DI u32x4 ldsread(unsigned addr) { return *(__attribute__((address_space(3))) u32x4*)addr; }

#define DMA(m0v, voff, rsrc, soff) \
    asm volatile("s_mov_b32 m0, %0\n\ts_nop 0\n\tbuffer_load_dwordx4 %1, %2, %3 offen" POL_KV " lds" :: "s"(m0v), "v"(voff), "s"(rsrc), "s"(soff) : "memory")
#define FRAG4(f, voff, rsrc, soff, POL) \
    asm volatile("buffer_load_dwordx4 %0, %4, %5, %6 offen" POL "\n\tbuffer_load_dwordx4 %1, %4, %5, %6 offen offset:64" POL "\n\t" \
                 "buffer_load_dwordx4 %2, %4, %5, %6 offen offset:128" POL "\n\tbuffer_load_dwordx4 %3, %4, %5, %6 offen offset:192" POL \
                 : "=&v"((f)[0]), "=&v"((f)[1]), "=&v"((f)[2]), "=&v"((f)[3]) : "v"(voff), "s"(rsrc), "s"(soff) : "memory")
#define PIN4(f) asm volatile("" : "+v"((f)[0]), "+v"((f)[1]), "+v"((f)[2]), "+v"((f)[3]))
#define WAITVM(n) asm volatile("s_waitcnt vmcnt(" #n ")" ::: "memory")
#define BAR() do { asm volatile("s_waitcnt lgkmcnt(0)" ::: "memory"); __builtin_amdgcn_s_barrier(); asm volatile("" ::: "memory"); } while (0)

constexpr float C2 = 0.08838834764831845f * 1.4426950408889634f;

struct Lane {
    unsigned kad[4];
    unsigned vad[8];
};

DI void make_bias(f32x4 (&bias)[5], int qb, int lane, int win, int ng) {
    const int q = qb + (lane & 15);
    const int lo = max(q - win, ng), hi = min(q + win, SEQ - 1);
    const unsigned span = (unsigned)(hi - lo);
    const int k0 = qb - 32 + 4 * (lane >> 4) - lo;
#pragma unroll
    for (int i = 0; i < 5; ++i)
#pragma unroll
        for (int r = 0; r < 4; ++r) bias[i][r] = (hi >= lo && (unsigned)(k0 + 16 * i + r) <= span) ? 0.f : -__builtin_inff();
}

template <int PH>
DI void local_phase(const Lane& ln, const u32x4 (&qf)[4], const u32x4 (&kg)[4], const u32x2 (&vg)[8], const f32x4 (&bias)[5], f32x4 biasg,
                    __amdgpu_buffer_rsrc_t ro, unsigned ovoff, unsigned osoff) {
    f32x4 s[5], sg = biasg;
#pragma unroll
    for (int t = 0; t < 4; ++t) sg = mfma(kg[t], qf[t], sg);
#pragma unroll
    for (int i = 0; i < 5; ++i) {
        s[i] = bias[i];
#pragma unroll
        for (int t = 0; t < 4; ++t) s[i] = mfma(ldsread(ln.kad[t] + (4 * PH + i) * TILEB), qf[t], s[i]);
    }
    float m = max3(sg[0], sg[1], s[0][0]), m2 = max3(s[0][1], s[0][2], s[0][3]);
#pragma unroll
    for (int i = 1; i < 5; ++i) { m = max3(m, s[i][0], s[i][1]); m2 = max3(m2, s[i][2], s[i][3]); }
    m = xmax(fmaxf(m, m2));
    const float mc = -m * C2;
    unsigned p[12];
#define EX(x) __builtin_amdgcn_exp2f(__builtin_fmaf((x), C2, mc))
    p[0] = pk(EX(sg[0]), EX(sg[1])); p[1] = 0;
#pragma unroll
    for (int i = 0; i < 5; ++i) { p[2 + 2 * i] = pk(EX(s[i][0]), EX(s[i][1])); p[3 + 2 * i] = pk(EX(s[i][2]), EX(s[i][3])); }
    const u32x4 pb0 = {p[0], p[1], p[2], p[3]}, pb1 = {p[4], p[5], p[6], p[7]}, pb2 = {p[8], p[9], p[10], p[11]};
    const u32x4 ones = {0x3F803F80u, 0x3F803F80u, 0x3F803F80u, 0x3F803F80u};
    f32x4 l = {0.f, 0.f, 0.f, 0.f};
    l = mfma(ones, pb0, l); l = mfma(ones, pb1, l); l = mfma(ones, pb2, l);
    const float rl = __builtin_amdgcn_rcpf(l[0]);
#pragma unroll
    for (int bp = 0; bp < 4; ++bp) {
        f32x4 o[2];
#pragma unroll
        for (int e = 0; e < 2; ++e) {
            const int b = 2 * bp + e;
            const unsigned a = ln.vad[b] + 4 * PH * TILEB;
            const u32x2 v0 = trread(a), v1 = trread(a + TILEB), v2 = trread(a + 2 * TILEB), v3 = trread(a + 3 * TILEB), v4 = trread(a + 4 * TILEB);
            f32x4 acc = {0.f, 0.f, 0.f, 0.f};
            acc = mfma((u32x4){vg[b][0], vg[b][1], v0[0], v0[1]}, pb0, acc);
            acc = mfma((u32x4){v1[0], v1[1], v2[0], v2[1]}, pb1, acc);
            acc = mfma((u32x4){v3[0], v3[1], v4[0], v4[1]}, pb2, acc);
            o[e] = acc;
        }
        auto x = __builtin_amdgcn_permlane16_swap(pk(o[0][0] * rl, o[0][1] * rl), pk(o[1][0] * rl, o[1][1] * rl), false, false);
        auto y = __builtin_amdgcn_permlane16_swap(pk(o[0][2] * rl, o[0][3] * rl), pk(o[1][2] * rl, o[1][3] * rl), false, false);
        __builtin_amdgcn_raw_buffer_store_b128((u32x4){x[0], y[0], x[1], y[1]}, ro, ovoff + 64 * bp, osoff, AUX_ST);
    }
}

DI void generic_rows(const __hip_bfloat16* q, const __hip_bfloat16* k, const __hip_bfloat16* v, __hip_bfloat16* o, int row0, int nrows, int win, int ng, int lane) {
    const unsigned* q2 = (const unsigned*)q; const unsigned* k2 = (const unsigned*)k; const unsigned* v2 = (const unsigned*)v; unsigned* o2 = (unsigned*)o;
    const int ngc = min(max(ng, 0), SEQ);
    const long long wl = win;
    for (int i = row0; i < row0 + nrows; ++i) {
        const unsigned qq = q2[i * 64 + lane];
        float q0 = __uint_as_float(qq << 16), q1 = __uint_as_float(qq & 0xffff0000u);
        int n0 = ngc, lo = (int)max((long long)i - wl, (long long)ngc), hi = (int)min((long long)i + wl, (long long)SEQ - 1);
        if (i < ngc) { n0 = SEQ; lo = 1; hi = 0; }
        if (wl < 0) { lo = 1; hi = 0; }
        if (n0 == 0 && hi < lo) { n0 = SEQ; q0 = 0.f; q1 = 0.f; }
        const int cnt = n0 + max(hi - lo + 1, 0);
        float m = -__builtin_inff(), l = 0.f, a0 = 0.f, a1 = 0.f;
        for (int n = 0; n < cnt; ++n) {
            const int j = n < n0 ? n : lo + (n - n0);
            const unsigned kk = k2[j * 64 + lane], vv = v2[j * 64 + lane];
            float s = q0 * __uint_as_float(kk << 16) + q1 * __uint_as_float(kk & 0xffff0000u);
#pragma unroll
            for (int x = 1; x < 64; x <<= 1) s += __shfl_xor(s, x);
            s *= C2;
            const float mn = fmaxf(m, s), f = __builtin_amdgcn_exp2f(m - mn), p = __builtin_amdgcn_exp2f(s - mn);
            l = l * f + p; a0 = a0 * f + p * __uint_as_float(vv << 16); a1 = a1 * f + p * __uint_as_float(vv & 0xffff0000u); m = mn;
        }
        o2[i * 64 + lane] = pk(a0 / l, a1 / l);
    }
}

__global__ __launch_bounds__(256, 2) void gl_attn(const __hip_bfloat16* __restrict__ Q, const __hip_bfloat16* __restrict__ K, const __hip_bfloat16* __restrict__ V,
                                                  __hip_bfloat16* __restrict__ O, const int* __restrict__ pwin, const int* __restrict__ png, char* __restrict__ ws) {
    __shared__ __attribute__((aligned(1024))) char lds[LDSB];
    const int tid = threadIdx.x, lane = tid & 63, w = __builtin_amdgcn_readfirstlane(tid >> 6);
    const int bid = blockIdx.x, idx = bid >> 3, head = (bid & 7) * (NH / 8) + idx / NCH, chunk = idx % NCH;
    const int r0 = chunk * CH;
    const int win = *pwin, ng = *png;
    const unsigned lb = (unsigned)(size_t)(lds_char*)lds;
    const __amdgpu_buffer_rsrc_t rq = __builtin_amdgcn_make_buffer_rsrc((char*)Q + (size_t)head * HEADB, 0, HEADB, 0x00020000);
    const __amdgpu_buffer_rsrc_t rk = __builtin_amdgcn_make_buffer_rsrc((char*)K + (size_t)head * HEADB, 0, HEADB, 0x00020000);
    const __amdgpu_buffer_rsrc_t rv = __builtin_amdgcn_make_buffer_rsrc((char*)V + (size_t)head * HEADB, 0, HEADB, 0x00020000);
    const __amdgpu_buffer_rsrc_t ro = __builtin_amdgcn_make_buffer_rsrc((char*)O + (size_t)head * HEADB, 0, HEADB, 0x00020000);
    const int g = lane >> 4, c16 = lane & 15;
    const int drow = 4 * w + g;
    const unsigned voffk = drow * ROWB + ((c16 ^ drow) << 4);
    const unsigned voffv = drow * ROWB + ((c16 ^ ((drow & 7) << 1)) << 4);
    const unsigned vofff = c16 * ROWB + (g << 4);
    const unsigned kvs = (unsigned)((r0 - 32) * ROWB);
    const unsigned mk = lb + KB + w * 1024, mv = lb + VB + w * 1024;
    const unsigned qs = (unsigned)((r0 + 16 * w) * ROWB);
    asm volatile("s_nop 4" ::: "memory");
#define LDK(T) DMA(mk + (T) * TILEB, voffk, rk, kvs + (T) * TILEB)
#define LDV(T) DMA(mv + (T) * TILEB, voffv, rv, kvs + (T) * TILEB)
    u32x4 q0[4], q1[4], q2[4], q3[4], kg[4], qg[4];
    LDK(0); LDK(1); LDK(2); LDK(3); LDK(4); LDK(5); LDK(6); LDK(7);
    FRAG4(q0, vofff, rq, qs, POL_Q);
    FRAG4(kg, vofff, rk, 0u, "");
    LDV(0); LDV(1); LDV(2); LDV(3); LDV(4); LDV(5); LDV(6); LDV(7);
    DMA(lb + VGB + w * 1024, voffv, rv, 0u);
    LDK(8); LDK(9); LDK(10); LDK(11);
    FRAG4(q1, vofff, rq, qs + 64 * ROWB, POL_Q);
    LDV(8); LDV(9); LDV(10); LDV(11);

    if (__builtin_expect(win < 0 || win > 32 || ng < 0 || ng > 2, 0)) {
        WAITVM(0);
        __builtin_amdgcn_sched_barrier(0);
        generic_rows(Q + (size_t)head * SEQ * HD, K + (size_t)head * SEQ * HD, V + (size_t)head * SEQ * HD, O + (size_t)head * SEQ * HD, r0 + 64 * w, 64, win, ng, lane);
        return;
    }

    Lane ln;
#pragma unroll
    for (int t = 0; t < 4; ++t) ln.kad[t] = lb + KB + w * TILEB + c16 * ROWB + (((4 * t + g) ^ c16) << 4);
    const int key = lane >> 2;
#pragma unroll
    for (int b = 0; b < 8; ++b) ln.vad[b] = lb + VB + w * TILEB + key * ROWB + ((b ^ (key & 7)) << 5) + ((lane & 3) << 3);
    f32x4 bias[5], biasg;
#pragma unroll
    for (int r = 0; r < 4; ++r) biasg[r] = (4 * g + r < ng) ? 0.f : -__builtin_inff();
    make_bias(bias, r0 + 16 * w, lane, win, ng);
    const unsigned ovoff = c16 * ROWB + ((g & 1) << 5) + ((g >> 1) << 4);
    const unsigned ovoff0 = (r0 + 16 * w + c16 < ng) ? 0x80000000u : ovoff;

    WAITVM(12); BAR(); PIN4(q0); PIN4(kg);
    LDK(12); LDK(13); LDK(14); LDK(15); LDK(16); LDK(17); LDK(18); LDK(19);
    LDV(12); LDV(13); LDV(14); LDV(15); LDV(16); LDV(17);
    FRAG4(qg, vofff, rq, 0u, "");
    unsigned ep;
    asm volatile("global_load_dword %0, %1, %2 sc1" : "=&v"(ep) : "v"(0u), "s"(ws + head * 256) : "memory");
    u32x2 vg[8];
#pragma unroll
    for (int b = 0; b < 8; ++b) vg[b] = trread(ln.vad[b] - w * TILEB + (VGB - VB));
    local_phase<0>(ln, q0, kg, vg, bias, biasg, ro, ovoff0, qs);
    make_bias(bias, r0 + 64 + 16 * w, lane, win, ng);
    WAITVM(23); BAR(); PIN4(q1);
    LDV(18); LDV(19);
    FRAG4(q2, vofff, rq, qs + 128 * ROWB, POL_Q);
    FRAG4(q3, vofff, rq, qs + 192 * ROWB, POL_Q);
    local_phase<1>(ln, q1, kg, vg, bias, biasg, ro, ovoff, qs + 64 * ROWB);
    WAITVM(12); BAR(); PIN4(qg);
    asm volatile("" : "+v"(ep));
    const unsigned tag = ep * 1664525u + 1013904223u;
    {
        f32x4 s[4];
#pragma unroll
        for (int i = 0; i < 4; ++i) {
            s[i] = (f32x4){0.f, 0.f, 0.f, 0.f};
#pragma unroll
            for (int t = 0; t < 4; ++t) s[i] = mfma(ldsread(ln.kad[t] + 3 * w * TILEB + (2 + i) * TILEB), qg[t], s[i]);
        }
        float m = max3(s[0][0], s[0][1], s[0][2]), m2 = max3(s[0][3], s[1][0], s[1][1]);
        m = max3(m, s[1][2], s[1][3]);
#pragma unroll
        for (int i = 2; i < 4; ++i) { m = max3(m, s[i][0], s[i][1]); m2 = max3(m2, s[i][2], s[i][3]); }
        m = xmax(fmaxf(m, m2));
        const float mc = -m * C2;
        unsigned p[8];
#pragma unroll
        for (int i = 0; i < 4; ++i) { p[2 * i] = pk(EX(s[i][0]), EX(s[i][1])); p[2 * i + 1] = pk(EX(s[i][2]), EX(s[i][3])); }
        const u32x4 pb0 = {p[0], p[1], p[2], p[3]}, pb1 = {p[4], p[5], p[6], p[7]};
        const u32x4 ones = {0x3F803F80u, 0x3F803F80u, 0x3F803F80u, 0x3F803F80u};
        f32x4 l = {0.f, 0.f, 0.f, 0.f};
        l = mfma(ones, pb0, l); l = mfma(ones, pb1, l);
        f32x4 og[8];
#pragma unroll
        for (int b = 0; b < 8; ++b) {
            const unsigned a = ln.vad[b] + 3 * w * TILEB + 2 * TILEB;
            const u32x2 v0 = trread(a), v1 = trread(a + TILEB), v2 = trread(a + 2 * TILEB), v3 = trread(a + 3 * TILEB);
            f32x4 acc = {0.f, 0.f, 0.f, 0.f};
            acc = mfma((u32x4){v0[0], v0[1], v1[0], v1[1]}, pb0, acc);
            acc = mfma((u32x4){v2[0], v2[1], v3[0], v3[1]}, pb1, acc);
            og[b] = acc;
        }
        if (c16 < 2) {
            const unsigned sa = lb + VB + ((w * 2 + c16) * 128 + 4 * g) * 4;
#pragma unroll
            for (int b = 0; b < 8; ++b) *(__attribute__((address_space(3))) f32x4*)(sa + 64 * b) = og[b];
            if (g == 0) *(__attribute__((address_space(3))) u32x2*)(lb + VB + 4096 + (w * 2 + c16) * 8) = (u32x2){__float_as_uint(-mc), __float_as_uint(l[0])};
        }
    }
    BAR();
    const int grow = w >> 1, gd = tid & 127;
    {
        float mw[4], lw[4], ow[4];
#pragma unroll
        for (int x = 0; x < 4; ++x) {
            const u32x2 ml = *(__attribute__((address_space(3))) u32x2*)(lb + VB + 4096 + (x * 2 + grow) * 8);
            mw[x] = __uint_as_float(ml[0]); lw[x] = __uint_as_float(ml[1]);
            ow[x] = *(__attribute__((address_space(3))) float*)(lb + VB + ((x * 2 + grow) * 128 + gd) * 4);
        }
        const float M = fmaxf(fmaxf(mw[0], mw[1]), fmaxf(mw[2], mw[3]));
        float L = 0.f, A = 0.f;
#pragma unroll
        for (int x = 0; x < 4; ++x) { const float f = __builtin_amdgcn_exp2f(mw[x] - M); L += f * lw[x]; A += f * ow[x]; }
        const float val = A * __builtin_amdgcn_rcpf(L), lse = M + __builtin_amdgcn_logf(L);
        const int slot = (head * NCH + chunk) * 2 + grow;
        __hip_atomic_store((gu64*)(ws + WS_O) + slot * 128 + gd, ((u64)tag << 32) | __float_as_uint(val), RLX_AGENT);
        __hip_atomic_store((gu64*)(ws + WS_LSE) + slot * 2 + (w & 1), ((u64)tag << 32) | __float_as_uint(lse), RLX_AGENT);
    }
    WAITVM(10); PIN4(q2);
    local_phase<2>(ln, q2, kg, vg, bias, biasg, ro, ovoff, qs + 128 * ROWB);
    make_bias(bias, r0 + 192 + 16 * w, lane, win, ng);
    WAITVM(10); PIN4(q3);
    if (chunk != 0) {
        local_phase<3>(ln, q3, kg, vg, bias, biasg, ro, ovoff, qs + 192 * ROWB);
        return;
    }
    gu64* go = (gu64*)(ws + WS_O) + (head * NCH * 2 + grow) * 128 + gd;
    gu64* gl = (gu64*)(ws + WS_LSE) + (head * NCH * 2 + grow) * 2 + (w & 1);
    u64 xo[NCH], xl[NCH];
#pragma unroll
    for (int c = 0; c < NCH; ++c) { xo[c] = __hip_atomic_load(go + c * 256, RLX_AGENT); xl[c] = __hip_atomic_load(gl + c * 4, RLX_AGENT); }
    local_phase<3>(ln, q3, kg, vg, bias, biasg, ro, ovoff, qs + 192 * ROWB);
    for (unsigned spins = 0; spins < (1u << 22); ++spins) {
        bool ok = true;
#pragma unroll
        for (int c = 0; c < NCH; ++c) ok &= (unsigned)(xo[c] >> 32) == tag && (unsigned)(xl[c] >> 32) == tag;
        if (__all(ok)) break;
        __builtin_amdgcn_s_sleep(2);
#pragma unroll
        for (int c = 0; c < NCH; ++c) { xo[c] = __hip_atomic_load(go + c * 256, RLX_AGENT); xl[c] = __hip_atomic_load(gl + c * 4, RLX_AGENT); }
    }
    float M = __uint_as_float((unsigned)xl[0]);
#pragma unroll
    for (int c = 1; c < NCH; ++c) M = fmaxf(M, __uint_as_float((unsigned)xl[c]));
    float L = 0.f, A = 0.f;
#pragma unroll
    for (int c = 0; c < NCH; ++c) { const float f = __builtin_amdgcn_exp2f(__uint_as_float((unsigned)xl[c]) - M); L += f; A += f * __uint_as_float((unsigned)xo[c]); }
    if (grow < ng) ((unsigned short*)O)[(size_t)head * SEQ * HD + grow * HD + gd] = (unsigned short)pk(A * __builtin_amdgcn_rcpf(L), 0.f);
    if (tid == 0) __hip_atomic_store((gu32*)(ws + head * 256), tag, RLX_AGENT);
}

}

extern "C" void kernel_launch(void* const* d_in, const int* in_sizes, int n_in, void* d_out, int out_size, void* d_ws, size_t ws_size,
                              hipStream_t stream) {
    gl_attn<<<NH * NCH, 256, 0, stream>>>((const __hip_bfloat16*)d_in[0], (const __hip_bfloat16*)d_in[1], (const __hip_bfloat16*)d_in[2],
                                          (__hip_bfloat16*)d_out, (const int*)d_in[3], (const int*)d_in[4], (char*)d_ws);
}
```

```cpp
#include <hip/hip_runtime.h>
#include <hip/hip_bf16.h>

namespace {

constexpr int NH = 32, SEQ = 2048, HD = 128;
constexpr int ROWB = HD * 2;
constexpr int HEADB = SEQ * ROWB;
constexpr int CH = 256;
constexpr int NCH = SEQ / CH;
constexpr int TILEB = 16 * ROWB;
constexpr int KB = 0;
constexpr int VB = 20 * TILEB;
constexpr int LDSB = 40 * TILEB;
constexpr int AUXS = KB + 19 * TILEB, AUXQ = AUXS + 2048, AUXK = AUXS + 2560, AUXV = AUXS + 3072, AUXF = AUXS + 3584;
constexpr int WS_FLG = 4096;
constexpr int WS_REC = 65536;
constexpr unsigned OOR = 0x80000000u;
constexpr int SC1 = 16;
constexpr float C2 = 0.08838834764831845f * 1.4426950408889634f;

typedef __bf16 bfx8 __attribute__((ext_vector_type(8)));
typedef short s16x4 __attribute__((ext_vector_type(4)));
typedef float f32x4 __attribute__((ext_vector_type(4)));
typedef float f32x2 __attribute__((ext_vector_type(2)));
typedef unsigned u32x4 __attribute__((ext_vector_type(4)));
typedef unsigned u32x2 __attribute__((ext_vector_type(2)));
typedef unsigned long long u64;
#define LDS(T, a) (*(__attribute__((address_space(3))) T*)(size_t)(a))
#define DI __device__ __forceinline__
#define SB() __builtin_amdgcn_sched_barrier(0)
#define SGB(m, n) __builtin_amdgcn_sched_group_barrier(m, n, 0)
#define WAITVM(n) asm volatile("s_waitcnt vmcnt(" #n ")" ::: "memory")
#define PIN4(f) asm volatile("" : "+v"((f)[0]), "+v"((f)[1]), "+v"((f)[2]), "+v"((f)[3]))

#define DMA4(m0v, v0, v1, v2, v3, rsrc, soff) \
    asm volatile("s_mov_b32 m0, %0\n\ts_nop 0\n\tbuffer_load_dwordx4 %1, %5, %6 offen lds\n\tbuffer_load_dwordx4 %2, %5, %6 offen offset:1024 lds\n\t" \
                 "buffer_load_dwordx4 %3, %5, %6 offen offset:2048 lds\n\tbuffer_load_dwordx4 %4, %5, %6 offen offset:3072 lds" \
                 :: "s"(m0v), "v"(v0), "v"(v1), "v"(v2), "v"(v3), "s"(rsrc), "s"(soff) : "memory")
#define DMA1(m0v, voff, rsrc, soff) \
    asm volatile("s_mov_b32 m0, %0\n\ts_nop 0\n\tbuffer_load_dwordx4 %1, %2, %3 offen lds" :: "s"(m0v), "v"(voff), "s"(rsrc), "s"(soff) : "memory")
#define FRAG4(f, voff, rsrc, soff) \
    asm volatile("buffer_load_dwordx4 %0, %4, %5, %6 offen\n\tbuffer_load_dwordx4 %1, %4, %5, %6 offen offset:64\n\t" \
                 "buffer_load_dwordx4 %2, %4, %5, %6 offen offset:128\n\tbuffer_load_dwordx4 %3, %4, %5, %6 offen offset:192" \
                 : "=&v"((f)[0]), "=&v"((f)[1]), "=&v"((f)[2]), "=&v"((f)[3]) : "v"(voff), "s"(rsrc), "s"(soff) : "memory")


DI f32x4 mfma(u32x4 a, u32x4 b, f32x4 c) {
    return __builtin_amdgcn_mfma_f32_16x16x32_bf16(__builtin_bit_cast(bfx8, a), __builtin_bit_cast(bfx8, b), c, 0, 0, 0);
}
DI unsigned pk(float lo, float hi) {
    typedef __bf16 bfx2 __attribute__((ext_vector_type(2)));
    bfx2 r = {(__bf16)lo, (__bf16)hi};
    return __builtin_bit_cast(unsigned, r);
}
DI unsigned expk(float a, float b, float mc) {
    const f32x2 t = __builtin_elementwise_fma((f32x2){a, b}, (f32x2){C2, C2}, (f32x2){mc, mc});
    return pk(__builtin_amdgcn_exp2f(t[0]), __builtin_amdgcn_exp2f(t[1]));
}
DI unsigned mulpk(float a, float b, float r) { const f32x2 t = (f32x2){a, b} * (f32x2){r, r}; return pk(t[0], t[1]); }
DI float max3(float a, float b, float c) { return fmaxf(fmaxf(a, b), c); }
DI float xmax(float m) {
    auto a = __builtin_amdgcn_permlane16_swap(__float_as_uint(m), __float_as_uint(m), false, false);
    m = fmaxf(__uint_as_float(a[0]), __uint_as_float(a[1]));
    auto b = __builtin_amdgcn_permlane32_swap(__float_as_uint(m), __float_as_uint(m), false, false);
    return fmaxf(__uint_as_float(b[0]), __uint_as_float(b[1]));
}
template <int C> DI unsigned dpp(unsigned x) { return __builtin_amdgcn_mov_dpp(x, C, 0xf, 0xf, true); }
template <int C> DI float dppf(float x) { return __uint_as_float(dpp<C>(__float_as_uint(x))); }
DI float sum8(float x) { x += dppf<0xB1>(x); x += dppf<0x4E>(x); return x + dppf<0x141>(x); }
DI float max8(float x) { x = fmaxf(x, dppf<0xB1>(x)); x = fmaxf(x, dppf<0x4E>(x)); return fmaxf(x, dppf<0x141>(x)); }
template <int N> DI float rotf(float x) { return dppf<0x130 - N>(x); }
template <int N> DI u32x4 rot4(u32x4 x) { return (u32x4){dpp<0x130 - N>(x[0]), dpp<0x130 - N>(x[1]), dpp<0x130 - N>(x[2]), dpp<0x130 - N>(x[3])}; }
template <> DI u32x4 rot4<0>(u32x4 x) { return x; }
DI u32x2 trread(unsigned addr) { return __builtin_bit_cast(u32x2, __builtin_amdgcn_ds_read_tr16_b64_v4i16(&LDS(s16x4, addr))); }
DI u32x4 pair(u32x2 a, u32x2 b) { return (u32x4){a[0], a[1], b[0], b[1]}; }

DI void flag_set(unsigned addr, unsigned val) { asm volatile("ds_write_b32 %0, %1" :: "v"(addr), "v"(val) : "memory"); }
template <int NAP = 1> DI void flag_wait(unsigned addr, unsigned need, unsigned val) {
    for (;;) {
        unsigned x;
        asm volatile("ds_read_b32 %0, %1\n\ts_waitcnt lgkmcnt(0)" : "=v"(x) : "v"(addr) : "memory");
        if (((unsigned)__ballot(x >= val) & need) == need) break;
        if (NAP) __builtin_amdgcn_s_sleep(NAP);
    }
}

DI void mask_init(f32x4 (&s)[5], int qb, int c16, int g, int win, int ng) {
    const int q = qb + c16, k0 = qb - 32 + 4 * g;
    const int lo = max(q - win, ng) - k0, hi = min(q + win, SEQ - 1) - k0;
#pragma unroll
    for (int i = 0; i < 5; ++i)
#pragma unroll
        for (int r = 0; r < 4; ++r) {
            const int c = 16 * i + r;
            s[i][r] = ((i > 2 || lo <= c) && (i < 2 || hi >= c)) ? 0.f : -__builtin_inff();
        }
}

DI void generic_rows(const unsigned* q2, const unsigned* k2, const unsigned* v2, unsigned* o2, int row0, int nrows, int win, int ng, int lane) {
    const int ngc = min(max(ng, 0), SEQ);
    const long long wl = win;
    for (int i = row0; i < row0 + nrows; ++i) {
        const unsigned qq = q2[i * 64 + lane];
        float q0 = __uint_as_float(qq << 16), q1 = __uint_as_float(qq & 0xffff0000u);
        int n0 = ngc, lo = (int)max((long long)i - wl, (long long)ngc), hi = (int)min((long long)i + wl, (long long)SEQ - 1);
        if (i < ngc) { n0 = SEQ; lo = 1; hi = 0; }
        if (wl < 0) { lo = 1; hi = 0; }
        if (n0 == 0 && hi < lo) { n0 = SEQ; q0 = 0.f; q1 = 0.f; }
        const int cnt = n0 + max(hi - lo + 1, 0);
        float m = -__builtin_inff(), l = 0.f, a0 = 0.f, a1 = 0.f;
        for (int n = 0; n < cnt; ++n) {
            const int j = n < n0 ? n : lo + (n - n0);
            const unsigned kk = k2[j * 64 + lane], vv = v2[j * 64 + lane];
            float s = q0 * __uint_as_float(kk << 16) + q1 * __uint_as_float(kk & 0xffff0000u);
#pragma unroll
            for (int x = 1; x < 64; x <<= 1) s += __shfl_xor(s, x);
            s *= C2;
            const float mn = fmaxf(m, s), f = __builtin_amdgcn_exp2f(m - mn), p = __builtin_amdgcn_exp2f(s - mn);
            l = l * f + p; a0 = a0 * f + p * __uint_as_float(vv << 16); a1 = a1 * f + p * __uint_as_float(vv & 0xffff0000u); m = mn;
        }
        o2[i * 64 + lane] = pk(a0 / l, a1 / l);
    }
}

__global__ __launch_bounds__(1024, 4) void gl_attn(const int* pwin, const void* Kp, const int* png, const void* Qp, char* ws, const void* Vp, void* Op) {
    __shared__ __attribute__((aligned(1024))) char lds[LDSB];
    const unsigned bid = blockIdx.x; const int tid = threadIdx.x;
    const u32x4 rk = {(unsigned)(size_t)Kp, (unsigned)((size_t)Kp >> 32), NH * HEADB, 0x00020000};
    const u32x4 rq = {(unsigned)(size_t)Qp, (unsigned)((size_t)Qp >> 32), NH * HEADB, 0x00020000};
    const u32x4 rv = {(unsigned)(size_t)Vp, (unsigned)((size_t)Vp >> 32), NH * HEADB, 0x00020000};
    const int lane = tid & 63, w = (unsigned)__builtin_amdgcn_readfirstlane(tid) >> 6;
    const unsigned blk = (bid & 7) * 32 + (bid >> 3);
    const unsigned head = blk / NCH, chunk = blk % NCH;
    const int r0 = chunk * CH;
    const unsigned lb = (unsigned)(size_t)(__attribute__((address_space(3))) char*)lds;
    const int g = lane >> 4, c16 = lane & 15;
    const unsigned fad = lb + AUXF + (lane & 31) * 4, fa_ = lb + AUXF + w * 4, fb_ = fa_ + 64;
    const unsigned vk0 = g * ROWB + ((c16 ^ g) << 4);
    const unsigned qs = (blk * 16 + w) * TILEB;
    const unsigned up = (qs + 2 * TILEB) | (((chunk * 16 + w + 2) & 128) << 24);
    const unsigned v1 = vk0 ^ 64, v2 = vk0 ^ 128, v3 = vk0 ^ 192, mk = lb + KB + (w + 4) * TILEB;
    const unsigned vf0 = c16 * ROWB + (g << 4);
    __builtin_amdgcn_s_setprio(3);
    asm volatile("" :: "v"(vk0), "v"(v1), "v"(v2), "v"(v3), "s"(up), "s"(mk));
    SB();
    int win, ng; unsigned ep;
    asm volatile("s_load_dword %0, %3, 0x0\n\ts_load_dword %1, %4, 0x0\n\ts_load_dword %2, %5, 0x0"
                 : "=&s"(win), "=&s"(ng), "=&s"(ep) : "s"(pwin), "s"(png), "s"(ws), "s"(Kp), "s"(Qp), "s"(Vp), "s"(Op) : "memory");
    u32x4 ka[4];
    if (w != 15) DMA4(mk, vk0, v1, v2, v3, rk, up);
    else FRAG4(ka, vf0, rk, up);
    const unsigned dn = ((blk * 16 + (w & 3) - 2) * TILEB) | ((chunk * 16 + (w & 3) - 2) & OOR);
    const unsigned vv0 = g * ROWB + ((c16 ^ (g << 1)) << 4);
    unsigned scrap0 = 0, scrap1 = 0, scrap2 = 0;
    if (w < 4) {
        DMA4(lb + KB + w * TILEB, vk0, v1, v2, v3, rk, dn);
        if (w == 0) {
            unsigned e = ep;
            asm volatile("s_waitcnt lgkmcnt(0)\n\ts_mul_i32 %0, %1, 0x19660d" : "=s"(e) : "s"(ep) : "memory");
            flag_set(lb + AUXF + lane * 4, lane < 32 ? 0u : e + 1013904223u);
            asm volatile("s_waitcnt lgkmcnt(0)" ::: "memory");
        } else {
            unsigned b0 = w == 1 ? rq[0] : rk[0], b1 = w == 1 ? rq[1] : rk[1];
            asm volatile("" : "+s"(b0), "+s"(b1));
            const u32x4 rg = {w == 3 ? rv[0] : b0, w == 3 ? rv[1] : b1, rk[2], rk[3]};
            if (lane < 32) DMA1(lb + AUXQ - 512 + w * 512, w == 3 ? vv0 : vk0, rg, head * HEADB);
        }
    } else if (w < 12) {
        u64 pc;
        asm volatile("s_getpc_b64 %0" : "=s"(pc));
        asm volatile("s_load_dword %0, %1, %2" : "=&s"(scrap0) : "s"(pc), "s"(((blk & 15) * 8 + w - 4) * 56) : "memory");
        asm volatile("s_load_dword %0, %2, %3\n\ts_load_dword %1, %2, %3 offset:0x200\n\ts_load_dword %1, %2, %3 offset:0x400\n\t"
                     "s_load_dword %0, %2, %3 offset:0x600\n\ts_load_dword %1, %2, %3 offset:0x800\n\ts_load_dword %1, %2, %3 offset:0xa00"
                     : "=&s"(scrap1), "=&s"(scrap2) : "s"(ws), "s"(WS_REC + blk * 4096 + (w - 4) * 64) : "memory");
    }
    asm volatile("s_barrier" ::: "memory");
#define VTILE(t, so) DMA4(lb + VB + (t) * TILEB, vv0, vv0 ^ 128, vv0, vv0 ^ 128, rv, so)
    if (w < 12) DMA4(lb + VB + (w + 8) * TILEB, vk0, v1, v2, v3, rq, qs);
    else { VTILE(w - 12, dn); VTILE(w - 8, qs - 10 * TILEB); }
    __builtin_amdgcn_s_setprio(1);
    asm volatile("s_waitcnt lgkmcnt(0)" : "+s"(win), "+s"(ng), "+s"(ep), "+s"(scrap0), "+s"(scrap1), "+s"(scrap2) :: "memory");
    if (__builtin_expect(win < 0 || win > 32 || ng < 0 || ng > 2, 0)) {
        WAITVM(0);
        SB();
        const size_t ho = (size_t)head * (HEADB / 4);
        generic_rows((const unsigned*)Qp + ho, (const unsigned*)Kp + ho, (const unsigned*)Vp + ho, (unsigned*)Op + ho, r0 + 16 * w, 16, win, ng, lane);
        return;
    }
    if (w >= 12) WAITVM(8); else WAITVM(4);
    PIN4(ka);
    flag_set(fa_, 1);
    unsigned tag = LDS(unsigned, lb + AUXF + 128);
    f32x4 s[5], sg;
    unsigned kad[4];
    mask_init(s, r0 + 16 * w, c16, g, win, ng);
#pragma unroll
    for (int r = 0; r < 4; ++r) sg[r] = (4 * g + r < ng) ? 0.f : -__builtin_inff();
#pragma unroll
    for (int t = 0; t < 4; ++t) kad[t] = lb + KB + w * TILEB + c16 * ROWB + (((4 * t + g) ^ c16) << 4);
    asm volatile("" : "+v"(s[0]), "+v"(s[1]), "+v"(s[2]), "+v"(s[3]), "+v"(s[4]), "+v"(sg), "+v"(kad[0]), "+v"(kad[1]), "+v"(kad[2]), "+v"(kad[3]), "+v"(tag));
    tag = __builtin_amdgcn_readfirstlane(tag);
    flag_wait(fad, ((31u << w) >> 4) | 15u, 1);
#define MM4(acc, f) { acc = mfma(f[0], qf[0], acc); acc = mfma(f[1], qf[1], acc); acc = mfma(f[2], qf[2], acc); acc = mfma(f[3], qf[3], acc); }
#define KRD(f, i) { f[0] = LDS(u32x4, kad[0] + (i) * TILEB); f[1] = LDS(u32x4, kad[1] + (i) * TILEB); f[2] = LDS(u32x4, kad[2] + (i) * TILEB); f[3] = LDS(u32x4, kad[3] + (i) * TILEB); }
    u32x4 kb[4], kg[4], qf[4];
    const unsigned ga = lb + AUXQ + (c16 & 1) * ROWB + ((g ^ (c16 & 1)) << 4);
    KRD(kb, 2)
    {
#pragma unroll
        for (int n = 0; n < 4; ++n) kg[n] = LDS(u32x4, ga + 64 * n);
        f32x4 t = {0.f, 0.f, 0.f, 0.f};
        t = mfma(kb[0], kg[0], t); t = mfma(kb[1], kg[1], t); t = mfma(kb[2], kg[2], t); t = mfma(kb[3], kg[3], t);
        if (c16 < 2) LDS(f32x4, lb + AUXS + c16 * 1024 + w * 64 + g * 16) = t;
        flag_set(fa_, 2);
    }
    const int key = lane >> 2;
    const unsigned vad0 = key * ROWB + ((key & 7) << 5) + ((lane & 3) << 3);
    const u32x4 ones = {0x3F803F80u, 0x3F803F80u, 0x3F803F80u, 0x3F803F80u};
    const __amdgpu_buffer_rsrc_t ro = __builtin_amdgcn_make_buffer_rsrc(Op, 0, NH * HEADB, 0x00020000);
    const __amdgpu_buffer_rsrc_t rw = __builtin_amdgcn_make_buffer_rsrc(ws + WS_REC + head * (NCH * 4096), 0, NCH * 4096, 0x00020000);
    const __amdgpu_buffer_rsrc_t rf = __builtin_amdgcn_make_buffer_rsrc(ws + WS_FLG, 0, 1024, 0x00020000);
    if (w >= 12) {
        flag_wait(fad, 0xffffu, 2);
        const unsigned as = lb + AUXS + (c16 & 1) * 1024 + (c16 >> 1) * 128 + g * 16;
        const f32x4 x = LDS(f32x4, as), y = LDS(f32x4, as + 64);
        float m = max3(max3(x[0], x[1], x[2]), x[3], y[0]);
        m = xmax(fmaxf(max3(m, y[1], y[2]), y[3]));
        m = fmaxf(m, rotf<2>(m)); m = fmaxf(m, rotf<4>(m)); m = fmaxf(m, rotf<8>(m));
        const float mc = -m * C2;
        u32x4 pg = {expk(x[0], x[1], mc), expk(x[2], x[3], mc), expk(y[0], y[1], mc), expk(y[2], y[3], mc)};
        float l = mfma(ones, pg, (f32x4){0.f, 0.f, 0.f, 0.f})[0];
        l += rotf<2>(l); l += rotf<4>(l); l += rotf<8>(l);
        float rlg = __builtin_amdgcn_rcpf(l), lse = __builtin_amdgcn_logf(l) - mc;
        unsigned a0 = lb + VB + 2 * TILEB + (vad0 ^ ((w - 12) << 6)), a1 = a0 ^ 32;
        unsigned off = c16 < 2 ? (chunk * 8 + c16 * 4 + g) * 16 : OOR;
        asm volatile("" : "+v"(pg), "+v"(rlg), "+v"(lse), "+v"(a0), "+v"(a1), "+v"(off));
        WAITVM(0);
        flag_set(fb_, 2);
        flag_wait(fad, 0x0fff0000u, 1);
        __builtin_amdgcn_s_setprio(2);
        FRAG4(qf, vf0, rq, qs);
        f32x4 acc = {0.f, 0.f, 0.f, 0.f}, acd = acc;
#define GSTEP(n, p) { const u32x4 x_ = pair(trread(a0 + 2 * (n) * TILEB), trread(a0 + (2 * (n) + 1) * TILEB)), y_ = pair(trread(a1 + 2 * (n) * TILEB), trread(a1 + (2 * (n) + 1) * TILEB)); \
        const u32x4 p_ = p; acc = mfma(x_, p_, acc); acd = mfma(y_, p_, acd); }
        flag_wait(fad, 0xf00f0000u, 2);
        GSTEP(0, pg) GSTEP(1, rot4<2>(pg)) SB(); GSTEP(2, rot4<4>(pg)) GSTEP(3, rot4<6>(pg)) SB(); GSTEP(4, rot4<8>(pg))
        u32x4 p5 = rot4<10>(pg), p6 = rot4<12>(pg), p7 = rot4<14>(pg);
        asm volatile("" : "+v"(acc), "+v"(acd), "+v"(p5), "+v"(p6), "+v"(p7));
        flag_wait<0>(fad, 0x03f00000u, 2);
        GSTEP(5, p5) GSTEP(6, p6) GSTEP(7, p7)
        SGB(0x100, 12); SGB(0x8, 6);
        const unsigned so = (w - 12) * 8192;
#define REC(a, b, o, so_) __builtin_amdgcn_raw_buffer_store_b128((u32x4){__float_as_uint(a), tag, __float_as_uint(b), tag}, rw, off + (o), so_, SC1)
        REC(lse, 0.f, 2048, so); REC(lse, 0.f, 2048, so + 4096);
        REC(acc[0] * rlg, acc[1] * rlg, 0, so); REC(acc[2] * rlg, acc[3] * rlg, 1024, so);
        REC(acd[0] * rlg, acd[1] * rlg, 0, so + 4096); REC(acd[2] * rlg, acd[3] * rlg, 1024, so + 4096);
        flag_set(fb_, 3);
        KRD(kb, 2)
    }
#pragma unroll
    for (int n = 0; n < 4; ++n) kg[n] = LDS(u32x4, ga + (AUXK - AUXQ) + 64 * n);
    if (w != 15) KRD(ka, 4)
    if (w >= 12) WAITVM(6); else WAITVM(0);
    PIN4(qf);
    if (w < 12) {
#pragma unroll
        for (int t = 0; t < 4; ++t) qf[t] = LDS(u32x4, kad[t] + (VB - KB + 8 * TILEB));
    }
    SB();
    MM4(s[2], kb) SB();
    KRD(kb, 0) MM4(sg, kg) SB();
    KRD(kg, 1) MM4(s[4], ka) SB();
    KRD(ka, 3) flag_set(fa_, 3);
    MM4(s[0], kb) SB();
    MM4(s[1], kg) MM4(s[3], ka)
    SB();
    u32x4 fa[8], f1[8], f2[8];
    u32x2 vg[8]; unsigned vad[8], oad[4];
#define VREAD(b) { fa[b] = pair(vg[b], trread(vad[b])); f1[b] = pair(trread(vad[b] + TILEB), trread(vad[b] + 2 * TILEB)); \
        f2[b] = pair(trread(vad[b] + 3 * TILEB), trread(vad[b] + 4 * TILEB)); }
    if (w < 12) {
        __builtin_amdgcn_s_setprio(2);
        if (w == 4) __builtin_amdgcn_raw_buffer_store_b32(tag, rf, lane * 1024, blk * 4, SC1);
        VTILE(w + 8, (qs + 6 * TILEB) | (((chunk * 16 + w + 6) & 128) << 24));
        flag_set(fb_, 1);
        __builtin_amdgcn_s_setprio(1);
    }
    u32x4 p0, p1, p2; float rl;
    {
        float m = max3(sg[0], sg[1], s[2][0]), m2 = max3(s[2][1], s[2][2], s[2][3]);
        m = max3(m, s[4][0], s[4][1]); m2 = max3(m2, s[4][2], s[4][3]);
        m = max3(m, s[0][0], s[0][1]); m2 = max3(m2, s[0][2], s[0][3]);
        m = max3(m, s[1][0], s[1][1]); m2 = max3(m2, s[1][2], s[1][3]);
        m = max3(m, s[3][0], s[3][1]); m2 = max3(m2, s[3][2], s[3][3]);
        const float mc = -xmax(fmaxf(m, m2)) * C2;
        p0 = (u32x4){expk(sg[0], sg[1], mc), 0u, expk(s[0][0], s[0][1], mc), expk(s[0][2], s[0][3], mc)};
        p1 = (u32x4){expk(s[1][0], s[1][1], mc), expk(s[1][2], s[1][3], mc), expk(s[2][0], s[2][1], mc), expk(s[2][2], s[2][3], mc)};
        p2 = (u32x4){expk(s[3][0], s[3][1], mc), expk(s[3][2], s[3][3], mc), expk(s[4][0], s[4][1], mc), expk(s[4][2], s[4][3], mc)};
        f32x4 l = {0.f, 0.f, 0.f, 0.f};
        l = mfma(ones, p0, l); l = mfma(ones, p1, l); l = mfma(ones, p2, l);
        rl = __builtin_amdgcn_rcpf(l[0]);
    }
    asm volatile("" : "+v"(p0), "+v"(p1), "+v"(p2), "+v"(rl));
    {
        unsigned vga = lb + AUXV + (vad0 & 0x13f), va = lb + VB + w * TILEB + vad0;
        unsigned oa = lb + KB + w * TILEB + c16 * ROWB + (((g >> 1) ^ (c16 & 7)) << 4) + (((g << 3) ^ c16) & 8);
        asm volatile("" : "+v"(vga), "+v"(va), "+v"(oa));
#pragma unroll
        for (int b = 0; b < 8; ++b) { vg[b] = trread(vga ^ (b << 5)); vad[b] = va ^ (b << 5); }
#pragma unroll
        for (int b = 0; b < 4; ++b) oad[b] = oa ^ (b << 5);
        asm volatile("" : "+v"(vad[0]), "+v"(vad[1]), "+v"(vad[2]), "+v"(vad[3]), "+v"(vad[4]), "+v"(vad[5]), "+v"(vad[6]), "+v"(vad[7]), "+v"(oad[0]), "+v"(oad[1]), "+v"(oad[2]), "+v"(oad[3]));
    }
    if (w < 12) { WAITVM(0); flag_set(fb_, 2); }
    flag_wait(fad, 0xf0000000u | ((31u << (w + 8)) & 0x0fff0000u) | ((31u << w) >> 4), (lane & 16) ? 2u : 3u);
    VREAD(0) VREAD(1) VREAD(2)
    SB();
    {
        f32x4 o[8];
#define PVMM(b) { o[b] = mfma(fa[b], p0, (f32x4){0.f, 0.f, 0.f, 0.f}); o[b] = mfma(f1[b], p1, o[b]); o[b] = mfma(f2[b], p2, o[b]); }
#define OWR(b) { LDS(u32x2, oad[(b) & 3] + ((b) >> 2) * 128) = (u32x2){mulpk(o[b][0], o[b][1], rl), mulpk(o[b][2], o[b][3], rl)}; }
#define WEAVE() { SGB(0x8, 1); SGB(0x100, 2); SGB(0x8, 1); SGB(0x100, 2); SGB(0x8, 1); SGB(0x100, 1); SGB(0x2, 4); SGB(0x200, 1); SB(); }
        VREAD(3) PVMM(0) WEAVE()
        VREAD(4) PVMM(1) OWR(0) WEAVE()
        VREAD(5) PVMM(2) OWR(1) WEAVE()
        VREAD(6) PVMM(3) OWR(2) WEAVE()
        VREAD(7) PVMM(4) OWR(3) WEAVE()
        PVMM(5) OWR(4) WEAVE()
        PVMM(6) OWR(5) WEAVE()
        PVMM(7) OWR(6) WEAVE()
        OWR(7)
    }
    u32x4 orow[4];
#pragma unroll
    for (int i = 0; i < 4; ++i) {
        const u32x4 x = LDS(u32x4, ((lb + KB + w * TILEB + vk0) ^ ((i & 1) << 6)) + i * 1024);
        orow[i] = i < 2 ? x : (u32x4){x[2], x[3], x[0], x[1]};
    }
    if (w == 0) {
        flag_wait(fad, 0x0f000000u, 2);
#pragma unroll 1
        for (unsigned spins = 0; spins < (1u << 20); ++spins) {
            u32x4 x;
            asm volatile("buffer_load_dwordx4 %0, %1, %2, 0 offen sc1\n\ts_waitcnt vmcnt(0)" : "=&v"(x) : "v"(lane * 16), "s"(rf) : "memory");
            if (__all(x[0] == tag && x[1] == tag && x[2] == tag && x[3] == tag)) break;
            __builtin_amdgcn_s_sleep(2);
        }
        __builtin_amdgcn_s_sleep(2);
        flag_set(fa_, 4);
        if (blk == 0 && lane == 0) __hip_atomic_store((unsigned*)ws, tag, __ATOMIC_RELAXED, __HIP_MEMORY_SCOPE_AGENT);
    }
    flag_wait(fad, 0xf0000001u, (lane & 16) ? 3u : 4u);
    if (w == 0) {
        u32x4 x0, x1; u32x2 x2;
#pragma unroll 1
        for (unsigned spins = 0; spins < (1u << 20); ++spins) {
            asm volatile("buffer_load_dwordx4 %0, %3, %4, %5 offen sc1\n\tbuffer_load_dwordx4 %1, %3, %4, %5 offen offset:1024 sc1\n\t"
                         "buffer_load_dwordx2 %2, %3, %4, %5 offen offset:2048 sc1\n\ts_waitcnt vmcnt(0)"
                         : "=&v"(x0), "=&v"(x1), "=&v"(x2) : "v"((lane & 7) * 128 + (lane >> 3) * 16), "s"(rw), "s"(chunk * 4096) : "memory");
            if (__all(x0[1] == tag && x0[3] == tag && x1[1] == tag && x1[3] == tag && x2[1] == tag)) break;
            __builtin_amdgcn_s_sleep(4);
        }
        const float ls = __uint_as_float(x2[0]);
        const float f = __builtin_amdgcn_exp2f(ls - max8(ls));
        const float r = __builtin_amdgcn_rcpf(sum8(f));
        const float a0 = sum8(f * __uint_as_float(x0[0])), a1 = sum8(f * __uint_as_float(x0[2]));
        const float a2 = sum8(f * __uint_as_float(x1[0])), a3 = sum8(f * __uint_as_float(x1[2]));
        const unsigned off = ((lane & 7) == 0 && (lane >> 5) < ng) ? (lane >> 5) * ROWB + chunk * 32 + (lane & 24) : OOR;
        __builtin_amdgcn_raw_buffer_store_b64((u32x2){mulpk(a0, a1, r), mulpk(a2, a3, r)}, ro, off, head * HEADB, SC1);
    }
    __builtin_amdgcn_raw_buffer_store_b128(orow[0], ro, (r0 + 16 * w + g < ng) ? OOR : lane * 16, qs, SC1);
#pragma unroll
    for (int i = 1; i < 4; ++i) __builtin_amdgcn_raw_buffer_store_b128(orow[i], ro, lane * 16 + i * 1024, qs, SC1);
}

}

extern "C" void kernel_launch(void* const* d_in, const int* in_sizes, int n_in, void* d_out, int out_size, void* d_ws, size_t ws_size,
                              hipStream_t stream) {
    gl_attn<<<NH * NCH, 1024, 0, stream>>>((const int*)d_in[3], d_in[1], (const int*)d_in[4], d_in[0], (char*)d_ws, d_in[2], d_out);
}
```

```cpp
#include <hip/hip_runtime.h>
#include <hip/hip_bf16.h>

namespace {

constexpr int NH = 32, SEQ = 2048, HD = 128;
constexpr int ROWB = HD * 2;
constexpr int HEADB = SEQ * ROWB;
constexpr int CH = 256;
constexpr int NCH = SEQ / CH;
constexpr int TILEB = 16 * ROWB;
constexpr int KB = 0;
constexpr int VB = 20 * TILEB;
constexpr int LDSB = 40 * TILEB;
constexpr int AUXS = KB + 19 * TILEB, AUXQ = AUXS + 2048, AUXK = AUXS + 2560, AUXV = AUXS + 3072, AUXF = AUXS + 3584;
constexpr int WS_FLG = 4096;
constexpr int WS_REC = 65536;
constexpr unsigned OOR = 0x80000000u;
constexpr int SC1 = 16;
constexpr float C2 = 0.08838834764831845f * 1.4426950408889634f;

typedef __bf16 bfx8 __attribute__((ext_vector_type(8)));
typedef short s16x4 __attribute__((ext_vector_type(4)));
typedef float f32x4 __attribute__((ext_vector_type(4)));
typedef float f32x2 __attribute__((ext_vector_type(2)));
typedef unsigned u32x4 __attribute__((ext_vector_type(4)));
typedef unsigned u32x2 __attribute__((ext_vector_type(2)));
typedef unsigned long long u64;
#define LDS(T, a) (*(__attribute__((address_space(3))) T*)(size_t)(a))
#define DI __device__ __forceinline__
#define SB() __builtin_amdgcn_sched_barrier(0)
#define SGB(m, n) __builtin_amdgcn_sched_group_barrier(m, n, 0)
#define WAITVM(n) asm volatile("s_waitcnt vmcnt(" #n ")" ::: "memory")

#define DMA4(m0v, v0, v1, v2, v3, rsrc, soff) \
    asm volatile("s_mov_b32 m0, %0\n\ts_nop 0\n\tbuffer_load_dwordx4 %1, %5, %6 offen lds\n\tbuffer_load_dwordx4 %2, %5, %6 offen offset:1024 lds\n\t" \
                 "buffer_load_dwordx4 %3, %5, %6 offen offset:2048 lds\n\tbuffer_load_dwordx4 %4, %5, %6 offen offset:3072 lds" \
                 :: "s"(m0v), "v"(v0), "v"(v1), "v"(v2), "v"(v3), "s"(rsrc), "s"(soff) : "memory")
#define DMA1(m0v, voff, rsrc, soff) \
    asm volatile("s_mov_b32 m0, %0\n\ts_nop 0\n\tbuffer_load_dwordx4 %1, %2, %3 offen lds" :: "s"(m0v), "v"(voff), "s"(rsrc), "s"(soff) : "memory")


DI f32x4 mfma(u32x4 a, u32x4 b, f32x4 c) {
    return __builtin_amdgcn_mfma_f32_16x16x32_bf16(__builtin_bit_cast(bfx8, a), __builtin_bit_cast(bfx8, b), c, 0, 0, 0);
}
DI unsigned pk(float lo, float hi) {
    typedef __bf16 bfx2 __attribute__((ext_vector_type(2)));
    bfx2 r = {(__bf16)lo, (__bf16)hi};
    return __builtin_bit_cast(unsigned, r);
}
DI unsigned expk(float a, float b, float mc) {
    const f32x2 t = __builtin_elementwise_fma((f32x2){a, b}, (f32x2){C2, C2}, (f32x2){mc, mc});
    return pk(__builtin_amdgcn_exp2f(t[0]), __builtin_amdgcn_exp2f(t[1]));
}
DI unsigned mulpk(float a, float b, float r) { const f32x2 t = (f32x2){a, b} * (f32x2){r, r}; return pk(t[0], t[1]); }
DI float max3(float a, float b, float c) { return fmaxf(fmaxf(a, b), c); }
DI float xmax(float m) {
    auto a = __builtin_amdgcn_permlane16_swap(__float_as_uint(m), __float_as_uint(m), false, false);
    m = fmaxf(__uint_as_float(a[0]), __uint_as_float(a[1]));
    auto b = __builtin_amdgcn_permlane32_swap(__float_as_uint(m), __float_as_uint(m), false, false);
    return fmaxf(__uint_as_float(b[0]), __uint_as_float(b[1]));
}
template <int C> DI unsigned dpp(unsigned x) { return __builtin_amdgcn_mov_dpp(x, C, 0xf, 0xf, true); }
template <int C> DI float dppf(float x) { return __uint_as_float(dpp<C>(__float_as_uint(x))); }
DI float sum8(float x) { x += dppf<0xB1>(x); x += dppf<0x4E>(x); return x + dppf<0x141>(x); }
DI float max8(float x) { x = fmaxf(x, dppf<0xB1>(x)); x = fmaxf(x, dppf<0x4E>(x)); return fmaxf(x, dppf<0x141>(x)); }
template <int N> DI float rotf(float x) { return dppf<0x130 - N>(x); }
template <int N> DI u32x4 rot4(u32x4 x) { return (u32x4){dpp<0x130 - N>(x[0]), dpp<0x130 - N>(x[1]), dpp<0x130 - N>(x[2]), dpp<0x130 - N>(x[3])}; }
DI u32x2 trread(unsigned addr) { return __builtin_bit_cast(u32x2, __builtin_amdgcn_ds_read_tr16_b64_v4i16(&LDS(s16x4, addr))); }
DI u32x4 pair(u32x2 a, u32x2 b) { return (u32x4){a[0], a[1], b[0], b[1]}; }

DI void flag_set(unsigned addr, unsigned val) { asm volatile("ds_write_b32 %0, %1" :: "v"(addr), "v"(val) : "memory"); }
DI void flag_wait(unsigned addr, unsigned need, unsigned val) {
    for (;;) {
        unsigned x;
        asm volatile("ds_read_b32 %0, %1\n\ts_waitcnt lgkmcnt(0)" : "=v"(x) : "v"(addr) : "memory");
        if (((unsigned)__ballot(x >= val) & need) == need) break;
        __builtin_amdgcn_s_sleep(1);
    }
}

DI void mask_init(f32x4 (&s)[5], int qb, int c16, int g, int win, int ng) {
    const int q = qb + c16, k0 = qb - 32 + 4 * g;
    const int lo = max(q - win, ng) - k0, hi = min(q + win, SEQ - 1) - k0;
#pragma unroll
    for (int i = 0; i < 5; ++i)
#pragma unroll
        for (int r = 0; r < 4; ++r) {
            const int c = 16 * i + r;
            s[i][r] = ((i > 2 || lo <= c) && (i < 2 || hi >= c)) ? 0.f : -__builtin_inff();
        }
}

DI void generic_rows(const unsigned* q2, const unsigned* k2, const unsigned* v2, unsigned* o2, int row0, int nrows, int win, int ng, int lane) {
    const int ngc = min(max(ng, 0), SEQ);
    const long long wl = win;
    for (int i = row0; i < row0 + nrows; ++i) {
        const unsigned qq = q2[i * 64 + lane];
        float q0 = __uint_as_float(qq << 16), q1 = __uint_as_float(qq & 0xffff0000u);
        int n0 = ngc, lo = (int)max((long long)i - wl, (long long)ngc), hi = (int)min((long long)i + wl, (long long)SEQ - 1);
        if (i < ngc) { n0 = SEQ; lo = 1; hi = 0; }
        if (wl < 0) { lo = 1; hi = 0; }
        if (n0 == 0 && hi < lo) { n0 = SEQ; q0 = 0.f; q1 = 0.f; }
        const int cnt = n0 + max(hi - lo + 1, 0);
        float m = -__builtin_inff(), l = 0.f, a0 = 0.f, a1 = 0.f;
        for (int n = 0; n < cnt; ++n) {
            const int j = n < n0 ? n : lo + (n - n0);
            const unsigned kk = k2[j * 64 + lane], vv = v2[j * 64 + lane];
            float s = q0 * __uint_as_float(kk << 16) + q1 * __uint_as_float(kk & 0xffff0000u);
#pragma unroll
            for (int x = 1; x < 64; x <<= 1) s += __shfl_xor(s, x);
            s *= C2;
            const float mn = fmaxf(m, s), f = __builtin_amdgcn_exp2f(m - mn), p = __builtin_amdgcn_exp2f(s - mn);
            l = l * f + p; a0 = a0 * f + p * __uint_as_float(vv << 16); a1 = a1 * f + p * __uint_as_float(vv & 0xffff0000u); m = mn;
        }
        o2[i * 64 + lane] = pk(a0 / l, a1 / l);
    }
}

__global__ __launch_bounds__(1024, 4) void gl_attn(const int* pwin, const void* Kp, const int* png, const void* Qp, char* ws, const void* Vp, void* Op) {
    __shared__ __attribute__((aligned(1024))) char lds[LDSB];
    const unsigned bid = blockIdx.x; const int tid = threadIdx.x;
    const u32x4 rk = {(unsigned)(size_t)Kp, (unsigned)((size_t)Kp >> 32), NH * HEADB, 0x00020000};
    const u32x4 rq = {(unsigned)(size_t)Qp, (unsigned)((size_t)Qp >> 32), NH * HEADB, 0x00020000};
    const u32x4 rv = {(unsigned)(size_t)Vp, (unsigned)((size_t)Vp >> 32), NH * HEADB, 0x00020000};
    const int lane = tid & 63, w = (unsigned)__builtin_amdgcn_readfirstlane(tid) >> 6;
    const unsigned blk = (bid & 7) * 32 + (bid >> 3);
    const unsigned head = blk / NCH, chunk = blk % NCH;
    const int r0 = chunk * CH;
    const unsigned lb = (unsigned)(size_t)(__attribute__((address_space(3))) char*)lds;
    const int g = lane >> 4, c16 = lane & 15;
    const unsigned fad = lb + AUXF + (lane & 31) * 4, fa_ = lb + AUXF + w * 4, fb_ = fa_ + 64;
    const unsigned vk0 = g * ROWB + ((c16 ^ g) << 4);
    const unsigned qs = (blk * 16 + w) * TILEB;
    const unsigned up = (qs + 2 * TILEB) | (((chunk * 16 + w + 2) & 128) << 24);
    const unsigned v1 = vk0 ^ 64, v2 = vk0 ^ 128, v3 = vk0 ^ 192, mk = lb + (w == 15 ? VB + 11 * TILEB : KB + (w + 4) * TILEB);
    __builtin_amdgcn_s_setprio(3);
    asm volatile("" :: "v"(vk0), "v"(v1), "v"(v2), "v"(v3), "s"(up), "s"(mk));
    SB();
    int win, ng; unsigned ep;
    asm volatile("s_load_dword %0, %3, 0x0\n\ts_load_dword %1, %4, 0x0\n\ts_load_dword %2, %5, 0x0"
                 : "=&s"(win), "=&s"(ng), "=&s"(ep) : "s"(pwin), "s"(png), "s"(ws), "s"(Kp), "s"(Qp), "s"(Vp), "s"(Op) : "memory");
    u32x4 ka[4];
    DMA4(mk, vk0, v1, v2, v3, rk, up);
    const unsigned dn = ((blk * 16 + (w & 3) - 2) * TILEB) | ((chunk * 16 + (w & 3) - 2) & OOR);
    const unsigned vv0 = g * ROWB + ((c16 ^ (g << 1)) << 4);
    unsigned scrap = 0;
    if (w < 4) {
        DMA4(lb + KB + w * TILEB, vk0, v1, v2, v3, rk, dn);
        if (w == 0) {
            unsigned e = ep;
            asm volatile("s_waitcnt lgkmcnt(0)\n\ts_mul_i32 %0, %1, 0x19660d" : "=s"(e) : "s"(ep) : "memory");
            flag_set(lb + AUXF + lane * 4, lane < 32 ? 0u : e + 1013904223u);
            asm volatile("s_waitcnt lgkmcnt(0)" ::: "memory");
        } else {
            unsigned b0 = w == 1 ? rq[0] : rk[0], b1 = w == 1 ? rq[1] : rk[1];
            asm volatile("" : "+s"(b0), "+s"(b1));
            const u32x4 rg = {w == 3 ? rv[0] : b0, w == 3 ? rv[1] : b1, rk[2], rk[3]};
            if (lane < 32) DMA1(lb + AUXQ - 512 + w * 512, w == 3 ? vv0 : vk0, rg, head * HEADB);
        }
    } else if (w < 12) {
        u64 pc;
        asm volatile("s_getpc_b64 %0" : "=s"(pc));
        asm volatile("s_load_dword %0, %1, %2" : "=&s"(scrap) : "s"(pc), "s"(((blk & 15) * 8 + w - 4) * 60) : "memory");
    }
    asm volatile("s_barrier" ::: "memory");
#define VTILE(t, so) DMA4(lb + VB + (t) * TILEB, vv0, vv0 ^ 128, vv0, vv0 ^ 128, rv, so)
    if (w < 8) DMA4(lb + VB + (w + 12) * TILEB, vk0, v1, v2, v3, rq, qs);
    else if (w < 12) VTILE(w - 8, dn);
    else {
        VTILE(w - 8, qs - 10 * TILEB);
        if (w == 15) {
            WAITVM(4);
#pragma unroll
            for (int t = 0; t < 4; ++t) ka[t] = LDS(u32x4, lb + VB + 11 * TILEB + c16 * ROWB + (((4 * t + g) ^ c16) << 4));
            asm volatile("s_waitcnt lgkmcnt(0)" : "+v"(ka[0]), "+v"(ka[1]), "+v"(ka[2]), "+v"(ka[3]) :: "memory");
        }
        VTILE(w - 4, qs - 6 * TILEB);
    }
    __builtin_amdgcn_s_setprio(1);
    asm volatile("s_waitcnt lgkmcnt(0)" : "+s"(win), "+s"(ng), "+s"(ep), "+s"(scrap) :: "memory");
    if (__builtin_expect(win < 0 || win > 32 || ng < 0 || ng > 2, 0)) {
        WAITVM(0);
        SB();
        const size_t ho = (size_t)head * (HEADB / 4);
        generic_rows((const unsigned*)Qp + ho, (const unsigned*)Kp + ho, (const unsigned*)Vp + ho, (unsigned*)Op + ho, r0 + 16 * w, 16, win, ng, lane);
        return;
    }
    if (w >= 12) WAITVM(8); else WAITVM(4);
    flag_set(fa_, 1);
    unsigned tag = LDS(unsigned, lb + AUXF + 128);
    f32x4 s[5], sg;
    unsigned kad[4];
    mask_init(s, r0 + 16 * w, c16, g, win, ng);
#pragma unroll
    for (int r = 0; r < 4; ++r) sg[r] = (4 * g + r < ng) ? 0.f : -__builtin_inff();
#pragma unroll
    for (int t = 0; t < 4; ++t) kad[t] = lb + KB + w * TILEB + c16 * ROWB + (((4 * t + g) ^ c16) << 4);
    asm volatile("" : "+v"(s[0]), "+v"(s[1]), "+v"(s[2]), "+v"(s[3]), "+v"(s[4]), "+v"(sg), "+v"(kad[0]), "+v"(kad[1]), "+v"(kad[2]), "+v"(kad[3]), "+v"(tag));
    tag = __builtin_amdgcn_readfirstlane(tag);
    flag_wait(fad, ((31u << w) >> 4) | 15u, 1);
#define MM4(acc, f) { acc = mfma(f[0], qf[0], acc); acc = mfma(f[1], qf[1], acc); acc = mfma(f[2], qf[2], acc); acc = mfma(f[3], qf[3], acc); }
#define KRD(f, i) { f[0] = LDS(u32x4, kad[0] + (i) * TILEB); f[1] = LDS(u32x4, kad[1] + (i) * TILEB); f[2] = LDS(u32x4, kad[2] + (i) * TILEB); f[3] = LDS(u32x4, kad[3] + (i) * TILEB); }
    u32x4 kb[4], kg[4], qf[4];
    const unsigned ga = lb + AUXQ + c16 * ROWB + ((g ^ c16) << 4);
    KRD(kb, 2)
    {
#pragma unroll
        for (int n = 0; n < 4; ++n) kg[n] = (u32x4){0u, 0u, 0u, 0u};
        if (c16 < 2) {
#pragma unroll
            for (int n = 0; n < 4; ++n) kg[n] = LDS(u32x4, ga + 64 * n);
        }
        f32x4 t = {0.f, 0.f, 0.f, 0.f};
        t = mfma(kb[0], kg[0], t); t = mfma(kb[1], kg[1], t); t = mfma(kb[2], kg[2], t); t = mfma(kb[3], kg[3], t);
        if (c16 < 2) LDS(f32x4, lb + AUXS + c16 * 1024 + w * 64 + g * 16) = t;
        flag_set(fa_, 2);
    }
    const int key = lane >> 2;
    const unsigned vad0 = key * ROWB + ((key & 7) << 5) + ((lane & 3) << 3);
    const u32x4 ones = {0x3F803F80u, 0x3F803F80u, 0x3F803F80u, 0x3F803F80u};
    const __amdgpu_buffer_rsrc_t ro = __builtin_amdgcn_make_buffer_rsrc(Op, 0, NH * HEADB, 0x00020000);
    const __amdgpu_buffer_rsrc_t rw = __builtin_amdgcn_make_buffer_rsrc(ws + WS_REC + head * (NCH * 4096), 0, NCH * 4096, 0x00020000);
    const __amdgpu_buffer_rsrc_t rf = __builtin_amdgcn_make_buffer_rsrc(ws + WS_FLG, 0, 256 * 64, 0x00020000);
    if (c16 < 2) {
#pragma unroll
        for (int n = 0; n < 4; ++n) kg[n] = LDS(u32x4, ga + (AUXK - AUXQ) + 64 * n);
    }
    if (w != 15) KRD(ka, 4)
    if (w >= 8) {
        flag_wait(fad, 0xffffu, 2);
        const unsigned as = lb + AUXS + (c16 & 1) * 1024 + (c16 >> 1) * 128 + g * 16;
        const f32x4 x = LDS(f32x4, as), y = LDS(f32x4, as + 64);
        float m = max3(max3(x[0], x[1], x[2]), x[3], y[0]);
        m = xmax(fmaxf(max3(m, y[1], y[2]), y[3]));
        m = fmaxf(m, rotf<2>(m)); m = fmaxf(m, rotf<4>(m)); m = fmaxf(m, rotf<8>(m));
        const float mc = -m * C2;
        u32x4 pg = {expk(x[0], x[1], mc), expk(x[2], x[3], mc), expk(y[0], y[1], mc), expk(y[2], y[3], mc)};
        float l = mfma(ones, pg, (f32x4){0.f, 0.f, 0.f, 0.f})[0];
        l += rotf<2>(l); l += rotf<4>(l); l += rotf<8>(l);
        float rlg = __builtin_amdgcn_rcpf(l), lse = __builtin_amdgcn_logf(l) - mc;
        unsigned a0 = lb + VB + 2 * TILEB + (vad0 ^ ((w - 8) << 5));
        unsigned off = c16 < 2 ? c16 * 4096 + (chunk * 8 + (w & 1) * 4 + g) * 16 : OOR;
        asm volatile("" : "+v"(pg), "+v"(rlg), "+v"(lse), "+v"(a0), "+v"(off));
        WAITVM(0);
        flag_set(fb_, 2);
        flag_wait(fad, 0x00ff0000u, 1);
        __builtin_amdgcn_s_setprio(2);
        if (w == 8) __builtin_amdgcn_raw_buffer_store_b32(tag, rf, lane < 16 ? lane * 4 : OOR, blk * 64, SC1);
        DMA4(lb + KB + (w - 8) * TILEB, vk0, v1, v2, v3, rq, qs);
        f32x4 acc = {0.f, 0.f, 0.f, 0.f};
#define GSTEP(n, p) acc = mfma(pair(trread(a0 + 2 * (n) * TILEB), trread(a0 + (2 * (n) + 1) * TILEB)), p, acc);
        flag_wait(fad, 0xff000000u, 2);
        GSTEP(0, pg) GSTEP(1, rot4<2>(pg)) GSTEP(2, rot4<4>(pg)) GSTEP(3, rot4<6>(pg)) GSTEP(4, rot4<8>(pg))
        u32x4 p5 = rot4<10>(pg), p6 = rot4<12>(pg), p7 = rot4<14>(pg);
        asm volatile("" : "+v"(acc), "+v"(p5), "+v"(p6), "+v"(p7));
        flag_wait(fad, 0x003f0000u, 2);
        GSTEP(5, p5) GSTEP(6, p6) GSTEP(7, p7)
        SGB(0x100, 6); SGB(0x8, 3);
#define REC(a, b, o) __builtin_amdgcn_raw_buffer_store_b128((u32x4){__float_as_uint(a), tag, __float_as_uint(b), tag}, rw, off + (o), (w & 6) * 4096, SC1)
        REC(lse, 0.f, 2048); REC(acc[0] * rlg, acc[1] * rlg, 0); REC(acc[2] * rlg, acc[3] * rlg, 1024);
        flag_set(fb_, 3);
        __builtin_amdgcn_s_setprio(1);
    }
    unsigned vad[8], vga = lb + AUXV + (vad0 & 0x13f), oa = lb + KB + w * TILEB + c16 * ROWB + (((g >> 1) ^ (c16 & 7)) << 4) + (((g << 3) ^ c16) & 8);
    {
        unsigned va = lb + VB + w * TILEB + vad0;
        asm volatile("" : "+v"(va));
#pragma unroll
        for (int b = 0; b < 8; ++b) vad[b] = va ^ (b << 5);
        asm volatile("" : "+v"(vad[0]), "+v"(vad[1]), "+v"(vad[2]), "+v"(vad[3]), "+v"(vad[4]), "+v"(vad[5]), "+v"(vad[6]), "+v"(vad[7]), "+v"(vga), "+v"(oa));
    }
    if (w >= 8) { flag_wait(fad, 0x00ff0000u, 2); WAITVM(3); } else WAITVM(0);
#pragma unroll
    for (int t = 0; t < 4; ++t) qf[t] = LDS(u32x4, kad[t] + (w < 8 ? VB - KB + 12 * TILEB : -8 * TILEB));
    SB();
#define NAP(x) { asm volatile("" : "+v"(x)); if (w < 8) __builtin_amdgcn_s_sleep(5); }
    MM4(s[2], kb) SB(); NAP(s[2])
    KRD(kb, 0) MM4(sg, kg) SB(); NAP(sg)
    KRD(kg, 1) MM4(s[4], ka) SB(); NAP(s[4])
    KRD(ka, 3) flag_set(fa_, 3);
    MM4(s[0], kb) SB(); NAP(s[0])
    MM4(s[1], kg) NAP(s[1]) MM4(s[3], ka)
    SB();
    u32x4 fa[8], f1[8], f2[8];
    u32x2 vg[8];
#define VREAD(b) { fa[b] = pair(vg[b], trread(vad[b])); f1[b] = pair(trread(vad[b] + TILEB), trread(vad[b] + 2 * TILEB)); \
        f2[b] = pair(trread(vad[b] + 3 * TILEB), trread(vad[b] + 4 * TILEB)); }
#pragma unroll
    for (int b = 0; b < 8; ++b) vg[b] = trread(vga ^ (b << 5));
    if (w < 8) {
        __builtin_amdgcn_s_setprio(2);
        VTILE(w + 12, (qs + 10 * TILEB) | (((chunk * 16 + w + 10) & 128) << 24));
        flag_set(fb_, 1);
        __builtin_amdgcn_s_setprio(1);
    } else { VREAD(0) VREAD(1) VREAD(2) }
    u32x4 p0, p1, p2; float rl;
    {
        float m = max3(sg[0], sg[1], s[2][0]), m2 = max3(s[2][1], s[2][2], s[2][3]);
        m = max3(m, s[4][0], s[4][1]); m2 = max3(m2, s[4][2], s[4][3]);
        m = max3(m, s[0][0], s[0][1]); m2 = max3(m2, s[0][2], s[0][3]);
        m = max3(m, s[1][0], s[1][1]); m2 = max3(m2, s[1][2], s[1][3]);
        m = max3(m, s[3][0], s[3][1]); m2 = max3(m2, s[3][2], s[3][3]);
        const float mc = -xmax(fmaxf(m, m2)) * C2;
        p0 = (u32x4){expk(sg[0], sg[1], mc), 0u, expk(s[0][0], s[0][1], mc), expk(s[0][2], s[0][3], mc)};
        p1 = (u32x4){expk(s[1][0], s[1][1], mc), expk(s[1][2], s[1][3], mc), expk(s[2][0], s[2][1], mc), expk(s[2][2], s[2][3], mc)};
        p2 = (u32x4){expk(s[3][0], s[3][1], mc), expk(s[3][2], s[3][3], mc), expk(s[4][0], s[4][1], mc), expk(s[4][2], s[4][3], mc)};
        f32x4 l = {0.f, 0.f, 0.f, 0.f};
        l = mfma(ones, p0, l); l = mfma(ones, p1, l); l = mfma(ones, p2, l);
        rl = __builtin_amdgcn_rcpf(l[0]);
    }
    asm volatile("" : "+v"(p0), "+v"(p1), "+v"(p2), "+v"(rl));
    if (w < 8) {
        if (w >= 4) { WAITVM(0); flag_set(fb_, 2); }
        flag_wait(fad, 0xff000000u, 2);
        VREAD(0) VREAD(1) VREAD(2)
    }
    SB();
    u32x2 op[8]; unsigned fl;
    {
        f32x4 o[8];
#define PVMM(b) { o[b] = mfma(fa[b], p0, (f32x4){0.f, 0.f, 0.f, 0.f}); o[b] = mfma(f1[b], p1, o[b]); o[b] = mfma(f2[b], p2, o[b]); }
#define OWR(b) { op[b] = (u32x2){mulpk(o[b][0], o[b][1], rl), mulpk(o[b][2], o[b][3], rl)}; }
#define WEAVE() { SGB(0x8, 1); SGB(0x100, 2); SGB(0x8, 1); SGB(0x100, 2); SGB(0x8, 1); SGB(0x100, 1); SGB(0x2, 4); SB(); }
        VREAD(3) PVMM(0) WEAVE()
        VREAD(4) PVMM(1) OWR(0) WEAVE()
        VREAD(5) PVMM(2) OWR(1) WEAVE()
        VREAD(6) PVMM(3) OWR(2) WEAVE()
        VREAD(7) PVMM(4) OWR(3) WEAVE()
        PVMM(5) OWR(4) WEAVE()
        asm volatile("ds_read_b32 %0, %1" : "=v"(fl) : "v"(fad) : "memory");
        PVMM(6) OWR(5) WEAVE()
        PVMM(7) OWR(6) WEAVE()
        OWR(7)
    }
    if (w < 4) { WAITVM(0); flag_set(fb_, 2); }
    asm volatile("s_waitcnt lgkmcnt(0)" : "+v"(fl), "+v"(op[0]), "+v"(op[1]), "+v"(op[2]), "+v"(op[3]), "+v"(op[4]), "+v"(op[5]), "+v"(op[6]), "+v"(op[7]));
    if (w == 0) {
        flag_wait(fad, 0xff000000u, 3);
        u32x4 x = {lane * 64u, lane * 64u + 4096, lane * 64u + 8192, lane * 64u + 12288};
#pragma unroll 1
        for (unsigned spins = 0; spins < (1u << 20); ++spins) {
            unsigned a, b, c, d;
            asm volatile("buffer_load_dword %0, %4, %8, 0 offen sc1\n\tbuffer_load_dword %1, %5, %8, 0 offen sc1\n\t"
                         "buffer_load_dword %2, %6, %8, 0 offen sc1\n\tbuffer_load_dword %3, %7, %8, 0 offen sc1\n\ts_waitcnt vmcnt(0)"
                         : "=&v"(a), "=&v"(b), "=&v"(c), "=&v"(d) : "v"(x[0]), "v"(x[1]), "v"(x[2]), "v"(x[3]), "s"(rf) : "memory");
            x = (u32x4){a == tag ? OOR : x[0], b == tag ? OOR : x[1], c == tag ? OOR : x[2], d == tag ? OOR : x[3]};
            if (__all((int)(x[0] & x[1] & x[2] & x[3]) < 0)) break;
            __builtin_amdgcn_s_sleep(2);
        }
        flag_set(fa_, 4);
        if (blk == 0 && lane == 0) __hip_atomic_store((unsigned*)ws, tag, __ATOMIC_RELAXED, __HIP_MEMORY_SCOPE_AGENT);
    }
    {
        const unsigned need = w < 8 ? 256u << w : (31u << w) >> 4;
        if (((unsigned)__ballot(fl >= 3) & need) != need) flag_wait(fad, need, 3);
    }
#pragma unroll
    for (int b = 0; b < 8; ++b) LDS(u32x2, (oa ^ ((b & 3) << 5)) + (b >> 2) * 128) = op[b];
    asm volatile("" ::: "memory");
    u32x4 orow[4];
#pragma unroll
    for (int i = 0; i < 4; ++i) {
        const u32x4 x = LDS(u32x4, ((lb + KB + w * TILEB + vk0) ^ ((i & 1) << 6)) + i * 1024);
        orow[i] = i < 2 ? x : (u32x4){x[2], x[3], x[0], x[1]};
    }
#define OSTORE() { if (w != 0 && __builtin_amdgcn_readfirstlane(fl) < 4) flag_wait(fad, 1u, 4); \
        __builtin_amdgcn_raw_buffer_store_b128(orow[0], ro, (r0 + 16 * w + g < ng) ? OOR : lane * 16, qs, SC1);     \
        _Pragma("unroll") for (int i = 1; i < 4; ++i) __builtin_amdgcn_raw_buffer_store_b128(orow[i], ro, lane * 16 + i * 1024, qs, SC1); }
    bool first = false;
    if (w == 7) {
        __builtin_amdgcn_s_sleep(6);
        asm volatile("ds_read_b32 %0, %1\n\ts_waitcnt lgkmcnt(0)" : "=v"(fl) : "v"(fad) : "memory");
        first = __builtin_amdgcn_readfirstlane(fl) < 4;
    }
    if (!first) OSTORE()
    if (w == 7) {
        u32x4 x0, x1; u32x2 x2;
#pragma unroll 1
        for (unsigned spins = 0; spins < (1u << 20); ++spins) {
            asm volatile("buffer_load_dwordx4 %0, %3, %4, %5 offen sc1\n\tbuffer_load_dwordx4 %1, %3, %4, %5 offen offset:1024 sc1\n\t"
                         "buffer_load_dwordx2 %2, %3, %4, %5 offen offset:2048 sc1\n\ts_waitcnt vmcnt(0)"
                         : "=&v"(x0), "=&v"(x1), "=&v"(x2) : "v"((lane & 7) * 128 + (lane >> 3) * 16), "s"(rw), "s"(chunk * 4096) : "memory");
            if (__all(x0[1] == tag && x0[3] == tag && x1[1] == tag && x1[3] == tag && x2[1] == tag)) break;
        }
        const float ls = __uint_as_float(x2[0]);
        const float f = __builtin_amdgcn_exp2f(ls - max8(ls));
        const float r = __builtin_amdgcn_rcpf(sum8(f));
        const float a0 = sum8(f * __uint_as_float(x0[0])), a1 = sum8(f * __uint_as_float(x0[2]));
        const float a2 = sum8(f * __uint_as_float(x1[0])), a3 = sum8(f * __uint_as_float(x1[2]));
        const unsigned off = ((lane & 7) == 0 && (int)(chunk & 1) < ng) ? lane : OOR;
        __builtin_amdgcn_raw_buffer_store_b64((u32x2){mulpk(a0, a1, r), mulpk(a2, a3, r)}, ro, off, head * HEADB + (chunk & 1) * ROWB + (chunk >> 1) * 64, SC1);
        if (first) OSTORE()
    }
}

}

extern "C" void kernel_launch(void* const* d_in, const int* in_sizes, int n_in, void* d_out, int out_size, void* d_ws, size_t ws_size,
                              hipStream_t stream) {
    gl_attn<<<NH * NCH, 1024, 0, stream>>>((const int*)d_in[3], d_in[1], (const int*)d_in[4], d_in[0], (char*)d_ws, d_in[2], d_out);
}
```

```cpp
#include <hip/hip_runtime.h>
#include <hip/hip_bf16.h>

namespace {

constexpr int NH = 32, SEQ = 2048, HD = 128;
constexpr int ROWB = HD * 2;
constexpr int HEADB = SEQ * ROWB;
constexpr int CH = 256;
constexpr int NCH = SEQ / CH;
constexpr int TILEB = 16 * ROWB;
constexpr int KB = 0;
constexpr int VB = 20 * TILEB;
constexpr int LDSB = 40 * TILEB;
constexpr int AUXS = KB + 19 * TILEB, AUXQ = AUXS + 2048, AUXK = AUXS + 2560, AUXV = AUXS + 3072, AUXF = AUXS + 3584;
constexpr int WS_FLG = 4096;
constexpr int WS_REC = 65536;
constexpr unsigned OOR = 0x80000000u;
constexpr int SC1 = 16;
constexpr float C2 = 0.08838834764831845f * 1.4426950408889634f;

typedef __bf16 bfx8 __attribute__((ext_vector_type(8)));
typedef short s16x4 __attribute__((ext_vector_type(4)));
typedef float f32x4 __attribute__((ext_vector_type(4)));
typedef float f32x2 __attribute__((ext_vector_type(2)));
typedef unsigned u32x4 __attribute__((ext_vector_type(4)));
typedef unsigned u32x2 __attribute__((ext_vector_type(2)));
typedef unsigned long long u64;
#define LDS(T, a) (*(__attribute__((address_space(3))) T*)(size_t)(a))
#define GLB(T, a) ((__attribute__((address_space(1))) T*)(a))
#define DI __device__ __forceinline__
#define SB() __builtin_amdgcn_sched_barrier(0)
#define SGB(m, n) __builtin_amdgcn_sched_group_barrier(m, n, 0)
#define WAITVM(n) asm volatile("s_waitcnt vmcnt(" #n ")" ::: "memory")

#define DMA4(m0v, v0, v1, v2, v3, rsrc, soff) \
    asm volatile("s_mov_b32 m0, %0\n\ts_nop 0\n\tbuffer_load_dwordx4 %1, %5, %6 offen lds\n\tbuffer_load_dwordx4 %2, %5, %6 offen offset:1024 lds\n\t" \
                 "buffer_load_dwordx4 %3, %5, %6 offen offset:2048 lds\n\tbuffer_load_dwordx4 %4, %5, %6 offen offset:3072 lds" \
                 :: "s"(m0v), "v"(v0), "v"(v1), "v"(v2), "v"(v3), "s"(rsrc), "s"(soff) : "memory")
#define DMA1(m0v, voff, rsrc, soff) \
    asm volatile("s_mov_b32 m0, %0\n\ts_nop 0\n\tbuffer_load_dwordx4 %1, %2, %3 offen lds" :: "s"(m0v), "v"(voff), "s"(rsrc), "s"(soff) : "memory")


DI f32x4 mfma(u32x4 a, u32x4 b, f32x4 c) {
    return __builtin_amdgcn_mfma_f32_16x16x32_bf16(__builtin_bit_cast(bfx8, a), __builtin_bit_cast(bfx8, b), c, 0, 0, 0);
}
DI unsigned pk(float lo, float hi) {
    typedef __bf16 bfx2 __attribute__((ext_vector_type(2)));
    bfx2 r = {(__bf16)lo, (__bf16)hi};
    return __builtin_bit_cast(unsigned, r);
}
DI unsigned expk(float a, float b, float mc) {
    const f32x2 t = __builtin_elementwise_fma((f32x2){a, b}, (f32x2){C2, C2}, (f32x2){mc, mc});
    return pk(__builtin_amdgcn_exp2f(t[0]), __builtin_amdgcn_exp2f(t[1]));
}
DI unsigned mulpk(float a, float b, float r) { const f32x2 t = (f32x2){a, b} * (f32x2){r, r}; return pk(t[0], t[1]); }
DI float max3(float a, float b, float c) { return fmaxf(fmaxf(a, b), c); }
DI float xmax(float m) {
    auto a = __builtin_amdgcn_permlane16_swap(__float_as_uint(m), __float_as_uint(m), false, false);
    m = fmaxf(__uint_as_float(a[0]), __uint_as_float(a[1]));
    auto b = __builtin_amdgcn_permlane32_swap(__float_as_uint(m), __float_as_uint(m), false, false);
    return fmaxf(__uint_as_float(b[0]), __uint_as_float(b[1]));
}
template <int C> DI unsigned dpp(unsigned x) { return __builtin_amdgcn_mov_dpp(x, C, 0xf, 0xf, true); }
template <int C> DI float dppf(float x) { return __uint_as_float(dpp<C>(__float_as_uint(x))); }
DI float sum8(float x) { x += dppf<0xB1>(x); x += dppf<0x4E>(x); return x + dppf<0x141>(x); }
DI float max8(float x) { x = fmaxf(x, dppf<0xB1>(x)); x = fmaxf(x, dppf<0x4E>(x)); return fmaxf(x, dppf<0x141>(x)); }
template <int N> DI float rotf(float x) { return dppf<0x130 - N>(x); }
template <int N> DI u32x4 rot4(u32x4 x) { return (u32x4){dpp<0x130 - N>(x[0]), dpp<0x130 - N>(x[1]), dpp<0x130 - N>(x[2]), dpp<0x130 - N>(x[3])}; }
DI u32x2 trread(unsigned addr) { return __builtin_bit_cast(u32x2, __builtin_amdgcn_ds_read_tr16_b64_v4i16(&LDS(s16x4, addr))); }
DI u32x4 pair(u32x2 a, u32x2 b) { return (u32x4){a[0], a[1], b[0], b[1]}; }

DI void flag_set(unsigned addr, unsigned val) { asm volatile("ds_write_b32 %0, %1" :: "v"(addr), "v"(val) : "memory"); }
DI void flag_wait(unsigned addr, unsigned need, unsigned val) {
    for (;;) {
        unsigned x;
        asm volatile("ds_read_b32 %0, %1\n\ts_waitcnt lgkmcnt(0)" : "=v"(x) : "v"(addr) : "memory");
        if (((unsigned)__ballot(x >= val) & need) == need) break;
        __builtin_amdgcn_s_sleep(1);
    }
}

DI void pace(u64 t0, unsigned d) {
#pragma unroll 1
    for (int it = 0; it < 1000; ++it) {
        u64 t;
        asm volatile("s_memrealtime %0\n\ts_waitcnt lgkmcnt(0)" : "=s"(t) :: "memory");
        if ((unsigned)(t - t0) >= d) break;
        __builtin_amdgcn_s_sleep(1);
    }
}

DI void mask_init(f32x4 (&s)[5], int qb, int c16, int g, int win, int ng) {
    const int q = qb + c16, k0 = qb - 32 + 4 * g;
    const int lo = max(q - win, ng) - k0, hi = min(q + win, SEQ - 1) - k0;
#pragma unroll
    for (int i = 0; i < 5; ++i)
#pragma unroll
        for (int r = 0; r < 4; ++r) {
            const int c = 16 * i + r;
            s[i][r] = ((i > 2 || lo <= c) && (i < 2 || hi >= c)) ? 0.f : -__builtin_inff();
        }
}

DI void generic_rows(const unsigned* q2, const unsigned* k2, const unsigned* v2, unsigned* o2, int row0, int nrows, int win, int ng, int lane) {
    const int ngc = min(max(ng, 0), SEQ);
    const long long wl = win;
    for (int i = row0; i < row0 + nrows; ++i) {
        const unsigned qq = q2[i * 64 + lane];
        float q0 = __uint_as_float(qq << 16), q1 = __uint_as_float(qq & 0xffff0000u);
        int n0 = ngc, lo = (int)max((long long)i - wl, (long long)ngc), hi = (int)min((long long)i + wl, (long long)SEQ - 1);
        if (i < ngc) { n0 = SEQ; lo = 1; hi = 0; }
        if (wl < 0) { lo = 1; hi = 0; }
        if (n0 == 0 && hi < lo) { n0 = SEQ; q0 = 0.f; q1 = 0.f; }
        const int cnt = n0 + max(hi - lo + 1, 0);
        float m = -__builtin_inff(), l = 0.f, a0 = 0.f, a1 = 0.f;
        for (int n = 0; n < cnt; ++n) {
            const int j = n < n0 ? n : lo + (n - n0);
            const unsigned kk = k2[j * 64 + lane], vv = v2[j * 64 + lane];
            float s = q0 * __uint_as_float(kk << 16) + q1 * __uint_as_float(kk & 0xffff0000u);
#pragma unroll
            for (int x = 1; x < 64; x <<= 1) s += __shfl_xor(s, x);
            s *= C2;
            const float mn = fmaxf(m, s), f = __builtin_amdgcn_exp2f(m - mn), p = __builtin_amdgcn_exp2f(s - mn);
            l = l * f + p; a0 = a0 * f + p * __uint_as_float(vv << 16); a1 = a1 * f + p * __uint_as_float(vv & 0xffff0000u); m = mn;
        }
        o2[i * 64 + lane] = pk(a0 / l, a1 / l);
    }
}

__global__ __launch_bounds__(1024, 4) void gl_attn(const void*, const void*, const void*, void*, void*, const int*, const int*) {
    __shared__ __attribute__((aligned(1024))) char lds[LDSB];
    const unsigned bid = blockIdx.x; const int tid = threadIdx.x;
    u64 Kp, Qp, Vp, Op, wsp, pwin, png;
    asm volatile("s_load_dwordx2 %0, %7, 0x0\n\ts_load_dwordx2 %1, %7, 0x8\n\ts_load_dwordx2 %2, %7, 0x10\n\ts_load_dwordx2 %3, %7, 0x18\n\t"
                 "s_load_dwordx2 %4, %7, 0x20\n\ts_load_dwordx2 %5, %7, 0x28\n\ts_load_dwordx2 %6, %7, 0x30"
                 : "=&s"(Kp), "=&s"(Qp), "=&s"(Vp), "=&s"(Op), "=&s"(wsp), "=&s"(pwin), "=&s"(png) : "s"(__builtin_amdgcn_kernarg_segment_ptr()) : "memory");
    SB();
    const int lane = tid & 63, w = (unsigned)__builtin_amdgcn_readfirstlane(tid) >> 6;
    const unsigned blk = (bid & 7) * 32 + (bid >> 3);
    const unsigned head = blk / NCH, chunk = blk % NCH;
    const int r0 = chunk * CH;
    const unsigned lb = (unsigned)(size_t)(__attribute__((address_space(3))) char*)lds;
    const int g = lane >> 4, c16 = lane & 15;
    const unsigned fad = lb + AUXF + (lane & 31) * 4, fa_ = lb + AUXF + w * 4, fb_ = fa_ + 64;
    const unsigned vk0 = g * ROWB + ((c16 ^ g) << 4);
    const unsigned qs = (blk * 16 + w) * TILEB;
    const unsigned up = (qs + 2 * TILEB) | (((chunk * 16 + w + 2) & 128) << 24);
    const unsigned v1 = vk0 ^ 64, v2 = vk0 ^ 128, v3 = vk0 ^ 192, mk = lb + (w == 15 ? VB + 11 * TILEB : KB + (w + 4) * TILEB);
    __builtin_amdgcn_s_setprio(3);
    asm volatile("" :: "v"(vk0), "v"(v1), "v"(v2), "v"(v3), "s"(up), "s"(mk));
    SB();
    asm volatile("s_waitcnt lgkmcnt(0)" : "+s"(Kp), "+s"(Qp), "+s"(Vp), "+s"(Op), "+s"(wsp), "+s"(pwin), "+s"(png) :: "memory");
    const u32x4 rk = {(unsigned)Kp, (unsigned)(Kp >> 32), NH * HEADB, 0x00020000};
    int win, ng; unsigned ep; u64 t0;
    asm volatile("s_memrealtime %3\n\ts_load_dword %0, %4, 0x0\n\ts_load_dword %1, %5, 0x0\n\ts_load_dword %2, %6, 0x0"
                 : "=&s"(win), "=&s"(ng), "=&s"(ep), "=&s"(t0) : "s"(pwin), "s"(png), "s"(wsp) : "memory");
    DMA4(mk, vk0, v1, v2, v3, rk, up);
    SB();
    const u32x4 rq = {(unsigned)Qp, (unsigned)(Qp >> 32), NH * HEADB, 0x00020000};
    const u32x4 rv = {(unsigned)Vp, (unsigned)(Vp >> 32), NH * HEADB, 0x00020000};
    char* ws = (char*)GLB(char, wsp);
    u32x4 ka[4];
    const unsigned dn = ((blk * 16 + (w & 3) - 2) * TILEB) | ((chunk * 16 + (w & 3) - 2) & OOR);
    const unsigned vv0 = g * ROWB + ((c16 ^ (g << 1)) << 4);
    unsigned scrap = 0;
    if (w < 4) {
        DMA4(lb + KB + w * TILEB, vk0, v1, v2, v3, rk, dn);
        if (w == 0) {
            unsigned e = ep;
            asm volatile("s_waitcnt lgkmcnt(0)\n\ts_mul_i32 %0, %1, 0x19660d" : "=s"(e) : "s"(ep) : "memory");
            flag_set(lb + AUXF + lane * 4, lane < 32 ? 0u : e + 1013904223u);
            asm volatile("s_waitcnt lgkmcnt(0)" ::: "memory");
        } else {
            unsigned b0 = w == 1 ? rq[0] : rk[0], b1 = w == 1 ? rq[1] : rk[1];
            asm volatile("" : "+s"(b0), "+s"(b1));
            const u32x4 rg = {w == 3 ? rv[0] : b0, w == 3 ? rv[1] : b1, rk[2], rk[3]};
            if (lane < 32) DMA1(lb + AUXQ - 512 + w * 512, w == 3 ? vv0 : vk0, rg, head * HEADB);
        }
    } else if (w < 12) {
        u64 pc;
        asm volatile("s_getpc_b64 %0" : "=s"(pc));
        asm volatile("s_load_dword %0, %1, %2" : "=&s"(scrap) : "s"(pc), "s"(((blk & 15) * 8 + w - 4) * 64) : "memory");
    }
    asm volatile("s_barrier" ::: "memory");
#define VTILE(t, so) DMA4(lb + VB + (t) * TILEB, vv0, vv0 ^ 128, vv0, vv0 ^ 128, rv, so)
    if (w < 8) DMA4(lb + VB + (w + 12) * TILEB, vk0, v1, v2, v3, rq, qs);
    else if (w < 12) VTILE(w - 8, dn);
    else {
        VTILE(w - 8, qs - 10 * TILEB);
        if (w == 15) {
            WAITVM(4);
#pragma unroll
            for (int t = 0; t < 4; ++t) ka[t] = LDS(u32x4, lb + VB + 11 * TILEB + c16 * ROWB + (((4 * t + g) ^ c16) << 4));
            asm volatile("s_waitcnt lgkmcnt(0)" : "+v"(ka[0]), "+v"(ka[1]), "+v"(ka[2]), "+v"(ka[3]) :: "memory");
        }
        VTILE(w - 4, qs - 6 * TILEB);
    }
    __builtin_amdgcn_s_setprio(1);
    asm volatile("s_waitcnt lgkmcnt(0)" : "+s"(win), "+s"(ng), "+s"(ep), "+s"(scrap), "+s"(t0) :: "memory");
    if (__builtin_expect(win < 0 || win > 32 || ng < 0 || ng > 2, 0)) {
        WAITVM(0);
        SB();
        const size_t ho = (size_t)head * (HEADB / 4);
        generic_rows((unsigned*)GLB(unsigned, Qp) + ho, (unsigned*)GLB(unsigned, Kp) + ho, (unsigned*)GLB(unsigned, Vp) + ho, (unsigned*)GLB(unsigned, Op) + ho, r0 + 16 * w, 16, win, ng, lane);
        return;
    }
    if (w >= 12) WAITVM(8); else WAITVM(4);
    flag_set(fa_, 1);
    unsigned tag = LDS(unsigned, lb + AUXF + 128);
    f32x4 s[5], sg;
    unsigned kad[4];
    mask_init(s, r0 + 16 * w, c16, g, win, ng);
#pragma unroll
    for (int r = 0; r < 4; ++r) sg[r] = (4 * g + r < ng) ? 0.f : -__builtin_inff();
#pragma unroll
    for (int t = 0; t < 4; ++t) kad[t] = lb + KB + w * TILEB + c16 * ROWB + (((4 * t + g) ^ c16) << 4);
    asm volatile("" : "+v"(s[0]), "+v"(s[1]), "+v"(s[2]), "+v"(s[3]), "+v"(s[4]), "+v"(sg), "+v"(kad[0]), "+v"(kad[1]), "+v"(kad[2]), "+v"(kad[3]), "+v"(tag));
    tag = __builtin_amdgcn_readfirstlane(tag);
    flag_wait(fad, ((31u << w) >> 4) | 15u, 1);
#define MM4(acc, f) { acc = mfma(f[0], qf[0], acc); acc = mfma(f[1], qf[1], acc); acc = mfma(f[2], qf[2], acc); acc = mfma(f[3], qf[3], acc); }
#define KRD(f, i) { f[0] = LDS(u32x4, kad[0] + (i) * TILEB); f[1] = LDS(u32x4, kad[1] + (i) * TILEB); f[2] = LDS(u32x4, kad[2] + (i) * TILEB); f[3] = LDS(u32x4, kad[3] + (i) * TILEB); }
    u32x4 kb[4], kg[4], qf[4];
    const unsigned ga = lb + AUXQ + c16 * ROWB + ((g ^ c16) << 4);
    KRD(kb, 2)
    {
#pragma unroll
        for (int n = 0; n < 4; ++n) kg[n] = (u32x4){0u, 0u, 0u, 0u};
        if (c16 < 2) {
#pragma unroll
            for (int n = 0; n < 4; ++n) kg[n] = LDS(u32x4, ga + 64 * n);
        }
        f32x4 t = {0.f, 0.f, 0.f, 0.f};
        t = mfma(kb[0], kg[0], t); t = mfma(kb[1], kg[1], t); t = mfma(kb[2], kg[2], t); t = mfma(kb[3], kg[3], t);
        if (c16 < 2) LDS(f32x4, lb + AUXS + c16 * 1024 + w * 64 + g * 16) = t;
        flag_set(fa_, 2);
    }
    const int key = lane >> 2;
    const unsigned vad0 = key * ROWB + ((key & 7) << 5) + ((lane & 3) << 3);
    const u32x4 ones = {0x3F803F80u, 0x3F803F80u, 0x3F803F80u, 0x3F803F80u};
    const __amdgpu_buffer_rsrc_t ro = __builtin_amdgcn_make_buffer_rsrc((char*)GLB(char, Op), 0, NH * HEADB, 0x00020000);
    const __amdgpu_buffer_rsrc_t rw = __builtin_amdgcn_make_buffer_rsrc(ws + WS_REC + head * (NCH * 4096), 0, NCH * 4096, 0x00020000);
    const __amdgpu_buffer_rsrc_t rf = __builtin_amdgcn_make_buffer_rsrc(ws + WS_FLG, 0, 256 * 64, 0x00020000);
    if (w >= 8) {
        flag_wait(fad, 0xffffu, 2);
        const unsigned as = lb + AUXS + (c16 & 1) * 1024 + (c16 >> 1) * 128 + g * 16;
        const f32x4 x = LDS(f32x4, as), y = LDS(f32x4, as + 64);
        float m = max3(max3(x[0], x[1], x[2]), x[3], y[0]);
        m = xmax(fmaxf(max3(m, y[1], y[2]), y[3]));
        m = fmaxf(m, rotf<2>(m)); m = fmaxf(m, rotf<4>(m)); m = fmaxf(m, rotf<8>(m));
        const float mc = -m * C2;
        u32x4 pg = {expk(x[0], x[1], mc), expk(x[2], x[3], mc), expk(y[0], y[1], mc), expk(y[2], y[3], mc)};
        float l = mfma(ones, pg, (f32x4){0.f, 0.f, 0.f, 0.f})[0];
        l += rotf<2>(l); l += rotf<4>(l); l += rotf<8>(l);
        float rlg = __builtin_amdgcn_rcpf(l), lse = __builtin_amdgcn_logf(l) - mc;
        unsigned a0 = lb + VB + 2 * TILEB + (vad0 ^ ((w - 8) << 5));
        unsigned off = c16 < 2 ? c16 * 4096 + (chunk * 8 + (w & 1) * 4 + g) * 16 : OOR;
        asm volatile("" : "+v"(pg), "+v"(rlg), "+v"(lse), "+v"(a0), "+v"(off));
        WAITVM(0);
        flag_set(fb_, 2);
        flag_wait(fad, 0x00ff0000u, 1);
        __builtin_amdgcn_s_setprio(2);
        if (w == 8) __builtin_amdgcn_raw_buffer_store_b32(tag, rf, lane < 16 ? lane * 4 : OOR, blk * 64, SC1);
        pace(t0, 445 + (w >> 2) * 50);
        DMA4(lb + KB + (w - 8) * TILEB, vk0, v1, v2, v3, rq, qs);
        f32x4 acc = {0.f, 0.f, 0.f, 0.f};
#define GSTEP(n, p) acc = mfma(pair(trread(a0 + 2 * (n) * TILEB), trread(a0 + (2 * (n) + 1) * TILEB)), p, acc);
        flag_wait(fad, 0xff000000u, 2);
        GSTEP(0, pg) GSTEP(1, rot4<2>(pg)) GSTEP(2, rot4<4>(pg)) GSTEP(3, rot4<6>(pg)) GSTEP(4, rot4<8>(pg))
        u32x4 p5 = rot4<10>(pg), p6 = rot4<12>(pg), p7 = rot4<14>(pg);
        asm volatile("" : "+v"(acc), "+v"(p5), "+v"(p6), "+v"(p7));
        flag_wait(fad, 0x003f0000u, 2);
        GSTEP(5, p5) GSTEP(6, p6) GSTEP(7, p7)
        SGB(0x100, 6); SGB(0x8, 3);
#define REC(a, b, o) __builtin_amdgcn_raw_buffer_store_b128((u32x4){__float_as_uint(a), tag, __float_as_uint(b), tag}, rw, off + (o), (w & 6) * 4096, SC1)
        REC(lse, 0.f, 2048); REC(acc[0] * rlg, acc[1] * rlg, 0); REC(acc[2] * rlg, acc[3] * rlg, 1024);
        flag_set(fb_, 3);
        __builtin_amdgcn_s_setprio(1);
    }
    if (c16 < 2) {
#pragma unroll
        for (int n = 0; n < 4; ++n) kg[n] = LDS(u32x4, ga + (AUXK - AUXQ) + 64 * n);
    }
    if (w != 15) KRD(ka, 4)
    unsigned vad[8], vga = lb + AUXV + (vad0 & 0x13f), oa = lb + KB + w * TILEB + c16 * ROWB + (((g >> 1) ^ (c16 & 7)) << 4) + (((g << 3) ^ c16) & 8);
    {
        unsigned va = lb + VB + w * TILEB + vad0;
        asm volatile("" : "+v"(va));
#pragma unroll
        for (int b = 0; b < 8; ++b) vad[b] = va ^ (b << 5);
        asm volatile("" : "+v"(vad[0]), "+v"(vad[1]), "+v"(vad[2]), "+v"(vad[3]), "+v"(vad[4]), "+v"(vad[5]), "+v"(vad[6]), "+v"(vad[7]), "+v"(vga), "+v"(oa));
    }
    if (w >= 8) { flag_wait(fad, 0x00ff0000u, 2); WAITVM(3); } else WAITVM(0);
#pragma unroll
    for (int t = 0; t < 4; ++t) qf[t] = LDS(u32x4, kad[t] + (w < 8 ? VB - KB + 12 * TILEB : -8 * TILEB));
    SB();
#define NAP(x) { asm volatile("" : "+v"(x)); if (w < 8) __builtin_amdgcn_s_sleep(5); }
    MM4(s[2], kb) SB(); NAP(s[2])
    KRD(kb, 0) MM4(sg, kg) SB(); NAP(sg)
    KRD(kg, 1) MM4(s[4], ka) SB(); NAP(s[4])
    KRD(ka, 3) flag_set(fa_, 3);
    MM4(s[0], kb) SB(); NAP(s[0])
    MM4(s[1], kg) NAP(s[1]) MM4(s[3], ka)
    SB();
    u32x4 fa[8], f1[8], f2[8];
    u32x2 vg[8];
#define VREAD(b) { fa[b] = pair(vg[b], trread(vad[b])); f1[b] = pair(trread(vad[b] + TILEB), trread(vad[b] + 2 * TILEB)); \
        f2[b] = pair(trread(vad[b] + 3 * TILEB), trread(vad[b] + 4 * TILEB)); }
#pragma unroll
    for (int b = 0; b < 8; ++b) vg[b] = trread(vga ^ (b << 5));
    if (w < 8) {
        pace(t0, 475 + (w >> 2) * 45);
        __builtin_amdgcn_s_setprio(2);
        VTILE(w + 12, (qs + 10 * TILEB) | (((chunk * 16 + w + 10) & 128) << 24));
        flag_set(fb_, 1);
        __builtin_amdgcn_s_setprio(1);
    } else { VREAD(0) VREAD(1) VREAD(2) }
    u32x4 p0, p1, p2; float rl;
    {
        float m = max3(sg[0], sg[1], s[2][0]), m2 = max3(s[2][1], s[2][2], s[2][3]);
        m = max3(m, s[4][0], s[4][1]); m2 = max3(m2, s[4][2], s[4][3]);
        m = max3(m, s[0][0], s[0][1]); m2 = max3(m2, s[0][2], s[0][3]);
        m = max3(m, s[1][0], s[1][1]); m2 = max3(m2, s[1][2], s[1][3]);
        m = max3(m, s[3][0], s[3][1]); m2 = max3(m2, s[3][2], s[3][3]);
        const float mc = -xmax(fmaxf(m, m2)) * C2;
        p0 = (u32x4){expk(sg[0], sg[1], mc), 0u, expk(s[0][0], s[0][1], mc), expk(s[0][2], s[0][3], mc)};
        p1 = (u32x4){expk(s[1][0], s[1][1], mc), expk(s[1][2], s[1][3], mc), expk(s[2][0], s[2][1], mc), expk(s[2][2], s[2][3], mc)};
        p2 = (u32x4){expk(s[3][0], s[3][1], mc), expk(s[3][2], s[3][3], mc), expk(s[4][0], s[4][1], mc), expk(s[4][2], s[4][3], mc)};
        f32x4 l = {0.f, 0.f, 0.f, 0.f};
        l = mfma(ones, p0, l); l = mfma(ones, p1, l); l = mfma(ones, p2, l);
        rl = __builtin_amdgcn_rcpf(l[0]);
    }
    asm volatile("" : "+v"(p0), "+v"(p1), "+v"(p2), "+v"(rl));
    if (w < 8) {
        if (w >= 4) { WAITVM(0); flag_set(fb_, 2); }
        flag_wait(fad, 0xff000000u, 2);
        VREAD(0) VREAD(1) VREAD(2)
    }
    SB();
    u32x2 op[8]; unsigned fl;
    {
        f32x4 o[8];
#define PVMM(b) { o[b] = mfma(fa[b], p0, (f32x4){0.f, 0.f, 0.f, 0.f}); o[b] = mfma(f1[b], p1, o[b]); o[b] = mfma(f2[b], p2, o[b]); }
#define OWR(b) { op[b] = (u32x2){mulpk(o[b][0], o[b][1], rl), mulpk(o[b][2], o[b][3], rl)}; }
#define WEAVE() { SGB(0x8, 1); SGB(0x100, 2); SGB(0x8, 1); SGB(0x100, 2); SGB(0x8, 1); SGB(0x100, 1); SGB(0x2, 4); SB(); }
        VREAD(3) PVMM(0) WEAVE()
        VREAD(4) PVMM(1) OWR(0) WEAVE()
        VREAD(5) PVMM(2) OWR(1) WEAVE()
        VREAD(6) PVMM(3) OWR(2) WEAVE()
        VREAD(7) PVMM(4) OWR(3) WEAVE()
        PVMM(5) OWR(4) WEAVE()
        asm volatile("ds_read_b32 %0, %1" : "=v"(fl) : "v"(fad) : "memory");
        PVMM(6) OWR(5) WEAVE()
        PVMM(7) OWR(6) WEAVE()
        OWR(7)
    }
    if (w < 4) { WAITVM(0); flag_set(fb_, 2); }
    asm volatile("s_waitcnt lgkmcnt(0)" : "+v"(fl), "+v"(op[0]), "+v"(op[1]), "+v"(op[2]), "+v"(op[3]), "+v"(op[4]), "+v"(op[5]), "+v"(op[6]), "+v"(op[7]));
    if (w == 0) {
        flag_wait(fad, 0xff000000u, 3);
        u32x4 x = {lane * 64u, lane * 64u + 4096, lane * 64u + 8192, lane * 64u + 12288};
#pragma unroll 1
        for (unsigned spins = 0; spins < (1u << 20); ++spins) {
            unsigned a, b, c, d;
            asm volatile("buffer_load_dword %0, %4, %8, 0 offen sc1\n\tbuffer_load_dword %1, %5, %8, 0 offen sc1\n\t"
                         "buffer_load_dword %2, %6, %8, 0 offen sc1\n\tbuffer_load_dword %3, %7, %8, 0 offen sc1\n\ts_waitcnt vmcnt(0)"
                         : "=&v"(a), "=&v"(b), "=&v"(c), "=&v"(d) : "v"(x[0]), "v"(x[1]), "v"(x[2]), "v"(x[3]), "s"(rf) : "memory");
            x = (u32x4){a == tag ? OOR : x[0], b == tag ? OOR : x[1], c == tag ? OOR : x[2], d == tag ? OOR : x[3]};
            if (__all((int)(x[0] & x[1] & x[2] & x[3]) < 0)) break;
            __builtin_amdgcn_s_sleep(2);
        }
        flag_set(fa_, 4);
        if (blk == 0 && lane == 0) __hip_atomic_store((unsigned*)ws, tag, __ATOMIC_RELAXED, __HIP_MEMORY_SCOPE_AGENT);
    }
    {
        const unsigned need = w < 8 ? 256u << w : (31u << w) >> 4;
        if (((unsigned)__ballot(fl >= 3) & need) != need) flag_wait(fad, need, 3);
    }
#pragma unroll
    for (int b = 0; b < 8; ++b) LDS(u32x2, (oa ^ ((b & 3) << 5)) + (b >> 2) * 128) = op[b];
    asm volatile("" ::: "memory");
    u32x4 orow[4];
#pragma unroll
    for (int i = 0; i < 4; ++i) {
        const u32x4 x = LDS(u32x4, ((lb + KB + w * TILEB + vk0) ^ ((i & 1) << 6)) + i * 1024);
        orow[i] = i < 2 ? x : (u32x4){x[2], x[3], x[0], x[1]};
    }
#define OSTORE() { if (w != 0 && __builtin_amdgcn_readfirstlane(fl) < 4) flag_wait(fad, 1u, 4); \
        __builtin_amdgcn_raw_buffer_store_b128(orow[0], ro, (r0 + 16 * w + g < ng) ? OOR : lane * 16, qs, SC1);     \
        _Pragma("unroll") for (int i = 1; i < 4; ++i) __builtin_amdgcn_raw_buffer_store_b128(orow[i], ro, lane * 16 + i * 1024, qs, SC1); }
    bool first = false;
    if (w == 7) {
        __builtin_amdgcn_s_sleep(6);
        asm volatile("ds_read_b32 %0, %1\n\ts_waitcnt lgkmcnt(0)" : "=v"(fl) : "v"(fad) : "memory");
        first = __builtin_amdgcn_readfirstlane(fl) < 4;
    }
    if (!first) OSTORE()
    if (w == 7) {
        u32x4 x0, x1; u32x2 x2;
#pragma unroll 1
        for (unsigned spins = 0; spins < (1u << 20); ++spins) {
            asm volatile("buffer_load_dwordx4 %0, %3, %4, %5 offen sc1\n\tbuffer_load_dwordx4 %1, %3, %4, %5 offen offset:1024 sc1\n\t"
                         "buffer_load_dwordx2 %2, %3, %4, %5 offen offset:2048 sc1\n\ts_waitcnt vmcnt(0)"
                         : "=&v"(x0), "=&v"(x1), "=&v"(x2) : "v"((lane & 7) * 128 + (lane >> 3) * 16), "s"(rw), "s"(chunk * 4096) : "memory");
            if (__all(x0[1] == tag && x0[3] == tag && x1[1] == tag && x1[3] == tag && x2[1] == tag)) break;
        }
        const float ls = __uint_as_float(x2[0]);
        const float f = __builtin_amdgcn_exp2f(ls - max8(ls));
        const float r = __builtin_amdgcn_rcpf(sum8(f));
        const float a0 = sum8(f * __uint_as_float(x0[0])), a1 = sum8(f * __uint_as_float(x0[2]));
        const float a2 = sum8(f * __uint_as_float(x1[0])), a3 = sum8(f * __uint_as_float(x1[2]));
        const unsigned off = ((lane & 7) == 0 && (int)(chunk & 1) < ng) ? lane : OOR;
        __builtin_amdgcn_raw_buffer_store_b64((u32x2){mulpk(a0, a1, r), mulpk(a2, a3, r)}, ro, off, head * HEADB + (chunk & 1) * ROWB + (chunk >> 1) * 64, SC1);
        if (first) OSTORE()
    }
}

}

extern "C" void kernel_launch(void* const* d_in, const int* in_sizes, int n_in, void* d_out, int out_size, void* d_ws, size_t ws_size,
                              hipStream_t stream) {
    gl_attn<<<NH * NCH, 1024, 0, stream>>>(d_in[1], d_in[0], d_in[2], d_out, d_ws, (const int*)d_in[3], (const int*)d_in[4]);
}
```

```cpp
#include <hip/hip_runtime.h>
#include <hip/hip_bf16.h>

namespace {

constexpr int NH = 32, SEQ = 2048, HD = 128;
constexpr int ROWB = HD * 2;
constexpr int HEADB = SEQ * ROWB;
constexpr int CH = 256;
constexpr int NCH = SEQ / CH;
constexpr int TILEB = 16 * ROWB;
constexpr int KB = 0;
constexpr int VB = 20 * TILEB;
constexpr int LDSB = 40 * TILEB;
constexpr int AUXS = KB + 19 * TILEB, AUXQ = AUXS + 2048, AUXK = AUXS + 2560, AUXV = AUXS + 3072, AUXF = AUXS + 3584;
constexpr int WS_FLG = 4096;
constexpr int WS_REC = 65536;
constexpr unsigned OOR = 0x80000000u;
constexpr int SC1 = 16;
constexpr float C2 = 0.08838834764831845f * 1.4426950408889634f;

typedef __bf16 bfx8 __attribute__((ext_vector_type(8)));
typedef short s16x4 __attribute__((ext_vector_type(4)));
typedef float f32x4 __attribute__((ext_vector_type(4)));
typedef float f32x2 __attribute__((ext_vector_type(2)));
typedef unsigned u32x4 __attribute__((ext_vector_type(4)));
typedef unsigned u32x2 __attribute__((ext_vector_type(2)));
typedef unsigned long long u64;
#define LDS(T, a) (*(__attribute__((address_space(3))) T*)(size_t)(a))
#define GLB(T, a) ((__attribute__((address_space(1))) T*)(a))
#define DI __device__ __forceinline__
#define SB() __builtin_amdgcn_sched_barrier(0)
#define SGB(m, n) __builtin_amdgcn_sched_group_barrier(m, n, 0)
#define WAITVM(n) asm volatile("s_waitcnt vmcnt(" #n ")" ::: "memory")

#define DMA4(m0v, v0, v1, v2, v3, rsrc, soff) \
    asm volatile("s_mov_b32 m0, %0\n\ts_nop 0\n\tbuffer_load_dwordx4 %1, %5, %6 offen lds\n\tbuffer_load_dwordx4 %2, %5, %6 offen offset:1024 lds\n\t" \
                 "buffer_load_dwordx4 %3, %5, %6 offen offset:2048 lds\n\tbuffer_load_dwordx4 %4, %5, %6 offen offset:3072 lds" \
                 :: "s"(m0v), "v"(v0), "v"(v1), "v"(v2), "v"(v3), "s"(rsrc), "s"(soff) : "memory")
#define DMA1(m0v, voff, rsrc, soff) \
    asm volatile("s_mov_b32 m0, %0\n\ts_nop 0\n\tbuffer_load_dwordx4 %1, %2, %3 offen lds" :: "s"(m0v), "v"(voff), "s"(rsrc), "s"(soff) : "memory")


DI f32x4 mfma(u32x4 a, u32x4 b, f32x4 c) {
    return __builtin_amdgcn_mfma_f32_16x16x32_bf16(__builtin_bit_cast(bfx8, a), __builtin_bit_cast(bfx8, b), c, 0, 0, 0);
}
DI unsigned pk(float lo, float hi) {
    typedef __bf16 bfx2 __attribute__((ext_vector_type(2)));
    bfx2 r = {(__bf16)lo, (__bf16)hi};
    return __builtin_bit_cast(unsigned, r);
}
DI unsigned expk(float a, float b, float mc) {
    const f32x2 t = __builtin_elementwise_fma((f32x2){a, b}, (f32x2){C2, C2}, (f32x2){mc, mc});
    return pk(__builtin_amdgcn_exp2f(t[0]), __builtin_amdgcn_exp2f(t[1]));
}
DI unsigned mulpk(float a, float b, float r) { const f32x2 t = (f32x2){a, b} * (f32x2){r, r}; return pk(t[0], t[1]); }
DI float max3(float a, float b, float c) { return fmaxf(fmaxf(a, b), c); }
DI float xmax(float m) {
    auto a = __builtin_amdgcn_permlane16_swap(__float_as_uint(m), __float_as_uint(m), false, false);
    m = fmaxf(__uint_as_float(a[0]), __uint_as_float(a[1]));
    auto b = __builtin_amdgcn_permlane32_swap(__float_as_uint(m), __float_as_uint(m), false, false);
    return fmaxf(__uint_as_float(b[0]), __uint_as_float(b[1]));
}
template <int C> DI unsigned dpp(unsigned x) { return __builtin_amdgcn_mov_dpp(x, C, 0xf, 0xf, true); }
template <int C> DI float dppf(float x) { return __uint_as_float(dpp<C>(__float_as_uint(x))); }
DI float sum8(float x) { x += dppf<0xB1>(x); x += dppf<0x4E>(x); return x + dppf<0x141>(x); }
DI float max8(float x) { x = fmaxf(x, dppf<0xB1>(x)); x = fmaxf(x, dppf<0x4E>(x)); return fmaxf(x, dppf<0x141>(x)); }
template <int N> DI float rotf(float x) { return dppf<0x130 - N>(x); }
template <int N> DI u32x4 rot4(u32x4 x) { return (u32x4){dpp<0x130 - N>(x[0]), dpp<0x130 - N>(x[1]), dpp<0x130 - N>(x[2]), dpp<0x130 - N>(x[3])}; }
DI u32x2 trread(unsigned addr) { return __builtin_bit_cast(u32x2, __builtin_amdgcn_ds_read_tr16_b64_v4i16(&LDS(s16x4, addr))); }
DI u32x4 pair(u32x2 a, u32x2 b) { return (u32x4){a[0], a[1], b[0], b[1]}; }

DI void flag_set(unsigned addr, unsigned val) { asm volatile("ds_write_b32 %0, %1" :: "v"(addr), "v"(val) : "memory"); }
DI void flag_wait(unsigned addr, unsigned need, unsigned val) {
    for (;;) {
        unsigned x;
        asm volatile("ds_read_b32 %0, %1\n\ts_waitcnt lgkmcnt(0)" : "=v"(x) : "v"(addr) : "memory");
        if (((unsigned)__ballot(x >= val) & need) == need) break;
        __builtin_amdgcn_s_sleep(1);
    }
}

DI void mask_init(f32x4 (&s)[5], int qb, int c16, int g, int win, int ng) {
    const int q = qb + c16, k0 = qb - 32 + 4 * g;
    const int lo = max(q - win, ng) - k0, hi = min(q + win, SEQ - 1) - k0;
#pragma unroll
    for (int i = 0; i < 5; ++i)
#pragma unroll
        for (int r = 0; r < 4; ++r) {
            const int c = 16 * i + r;
            s[i][r] = ((i > 2 || lo <= c) && (i < 2 || hi >= c)) ? 0.f : -__builtin_inff();
        }
}

DI void generic_rows(const unsigned* q2, const unsigned* k2, const unsigned* v2, unsigned* o2, int row0, int nrows, int win, int ng, int lane) {
    const int ngc = min(max(ng, 0), SEQ);
    const long long wl = win;
    for (int i = row0; i < row0 + nrows; ++i) {
        const unsigned qq = q2[i * 64 + lane];
        float q0 = __uint_as_float(qq << 16), q1 = __uint_as_float(qq & 0xffff0000u);
        int n0 = ngc, lo = (int)max((long long)i - wl, (long long)ngc), hi = (int)min((long long)i + wl, (long long)SEQ - 1);
        if (i < ngc) { n0 = SEQ; lo = 1; hi = 0; }
        if (wl < 0) { lo = 1; hi = 0; }
        if (n0 == 0 && hi < lo) { n0 = SEQ; q0 = 0.f; q1 = 0.f; }
        const int cnt = n0 + max(hi - lo + 1, 0);
        float m = -__builtin_inff(), l = 0.f, a0 = 0.f, a1 = 0.f;
        for (int n = 0; n < cnt; ++n) {
            const int j = n < n0 ? n : lo + (n - n0);
            const unsigned kk = k2[j * 64 + lane], vv = v2[j * 64 + lane];
            float s = q0 * __uint_as_float(kk << 16) + q1 * __uint_as_float(kk & 0xffff0000u);
#pragma unroll
            for (int x = 1; x < 64; x <<= 1) s += __shfl_xor(s, x);
            s *= C2;
            const float mn = fmaxf(m, s), f = __builtin_amdgcn_exp2f(m - mn), p = __builtin_amdgcn_exp2f(s - mn);
            l = l * f + p; a0 = a0 * f + p * __uint_as_float(vv << 16); a1 = a1 * f + p * __uint_as_float(vv & 0xffff0000u); m = mn;
        }
        o2[i * 64 + lane] = pk(a0 / l, a1 / l);
    }
}

__global__ __launch_bounds__(1024, 4) void gl_attn(const void*, const void*, const void*, void*, void*, const int*, const int*) {
    __shared__ __attribute__((aligned(1024))) char lds[LDSB];
    const unsigned bid = blockIdx.x; const int tid = threadIdx.x;
    u64 Kp, Qp, Vp, Op, wsp, pwin, png;
    asm volatile("s_load_dwordx2 %0, %7, 0x0\n\ts_load_dwordx2 %1, %7, 0x8\n\ts_load_dwordx2 %2, %7, 0x10\n\ts_load_dwordx2 %3, %7, 0x18\n\t"
                 "s_load_dwordx2 %4, %7, 0x20\n\ts_load_dwordx2 %5, %7, 0x28\n\ts_load_dwordx2 %6, %7, 0x30"
                 : "=&s"(Kp), "=&s"(Qp), "=&s"(Vp), "=&s"(Op), "=&s"(wsp), "=&s"(pwin), "=&s"(png) : "s"(__builtin_amdgcn_kernarg_segment_ptr()) : "memory");
    SB();
    const int lane = tid & 63, w = (unsigned)__builtin_amdgcn_readfirstlane(tid) >> 6;
    const unsigned blk = (bid & 7) * 32 + (bid >> 3);
    const unsigned head = blk / NCH, chunk = blk % NCH;
    const int r0 = chunk * CH;
    const unsigned lb = (unsigned)(size_t)(__attribute__((address_space(3))) char*)lds;
    const int g = lane >> 4, c16 = lane & 15;
    const unsigned fad = lb + AUXF + (lane & 31) * 4, fa_ = lb + AUXF + w * 4, fb_ = fa_ + 64;
    const unsigned vk0 = g * ROWB + ((c16 ^ g) << 4);
    const unsigned qs = (blk * 16 + w) * TILEB;
    const unsigned up = (qs + 2 * TILEB) | (((chunk * 16 + w + 2) & 128) << 24);
    const unsigned v1 = vk0 ^ 64, v2 = vk0 ^ 128, v3 = vk0 ^ 192, mk = lb + (w == 15 ? VB + 11 * TILEB : KB + (w + 4) * TILEB);
    __builtin_amdgcn_s_setprio(3);
    asm volatile("" :: "v"(vk0), "v"(v1), "v"(v2), "v"(v3), "s"(up), "s"(mk));
    SB();
    asm volatile("s_waitcnt lgkmcnt(0)" : "+s"(Kp), "+s"(Qp), "+s"(Vp), "+s"(Op), "+s"(wsp), "+s"(pwin), "+s"(png) :: "memory");
    const u32x4 rk = {(unsigned)Kp, (unsigned)(Kp >> 32), NH * HEADB, 0x00020000};
    int win, ng; unsigned ep;
    asm volatile("s_load_dword %0, %3, 0x0\n\ts_load_dword %1, %4, 0x0\n\ts_load_dword %2, %5, 0x0"
                 : "=&s"(win), "=&s"(ng), "=&s"(ep) : "s"(pwin), "s"(png), "s"(wsp) : "memory");
    DMA4(mk, vk0, v1, v2, v3, rk, up);
    SB();
    const u32x4 rq = {(unsigned)Qp, (unsigned)(Qp >> 32), NH * HEADB, 0x00020000};
    const u32x4 rv = {(unsigned)Vp, (unsigned)(Vp >> 32), NH * HEADB, 0x00020000};
    char* ws = (char*)GLB(char, wsp);
    u32x4 ka[4];
    const unsigned dn = ((blk * 16 + (w & 3) - 2) * TILEB) | ((chunk * 16 + (w & 3) - 2) & OOR);
    const unsigned vv0 = g * ROWB + ((c16 ^ (g << 1)) << 4);
    unsigned scrap = 0;
    if (w < 4) {
        DMA4(lb + KB + w * TILEB, vk0, v1, v2, v3, rk, dn);
        if (w == 0) {
            unsigned e = ep;
            asm volatile("s_waitcnt lgkmcnt(0)\n\ts_mul_i32 %0, %1, 0x19660d" : "=s"(e) : "s"(ep) : "memory");
            flag_set(lb + AUXF + lane * 4, lane < 32 ? 0u : e + 1013904223u);
            asm volatile("s_waitcnt lgkmcnt(0)" ::: "memory");
        } else {
            unsigned b0 = w == 1 ? rq[0] : rk[0], b1 = w == 1 ? rq[1] : rk[1];
            asm volatile("" : "+s"(b0), "+s"(b1));
            const u32x4 rg = {w == 3 ? rv[0] : b0, w == 3 ? rv[1] : b1, rk[2], rk[3]};
            if (lane < 32) DMA1(lb + AUXQ - 512 + w * 512, w == 3 ? vv0 : vk0, rg, head * HEADB);
        }
    } else if (w < 12) {
        u64 pc;
        asm volatile("s_getpc_b64 %0" : "=s"(pc));
        asm volatile("s_load_dword %0, %1, %2" : "=&s"(scrap) : "s"(pc), "s"(((blk & 15) * 8 + w - 4) * 60) : "memory");
    }
    asm volatile("s_barrier" ::: "memory");
#define VTILE(t, so) DMA4(lb + VB + (t) * TILEB, vv0, vv0 ^ 128, vv0, vv0 ^ 128, rv, so)
    if (w < 8) DMA4(lb + VB + (w + 12) * TILEB, vk0, v1, v2, v3, rq, qs);
    else if (w < 12) VTILE(w - 8, dn);
    else {
        VTILE(w - 8, qs - 10 * TILEB);
        if (w == 15) {
            WAITVM(4);
#pragma unroll
            for (int t = 0; t < 4; ++t) ka[t] = LDS(u32x4, lb + VB + 11 * TILEB + c16 * ROWB + (((4 * t + g) ^ c16) << 4));
            asm volatile("s_waitcnt lgkmcnt(0)" : "+v"(ka[0]), "+v"(ka[1]), "+v"(ka[2]), "+v"(ka[3]) :: "memory");
        }
        VTILE(w - 4, qs - 6 * TILEB);
    }
    __builtin_amdgcn_s_setprio(1);
    asm volatile("s_waitcnt lgkmcnt(0)" : "+s"(win), "+s"(ng), "+s"(ep), "+s"(scrap) :: "memory");
    if (__builtin_expect(win < 0 || win > 32 || ng < 0 || ng > 2, 0)) {
        WAITVM(0);
        SB();
        const size_t ho = (size_t)head * (HEADB / 4);
        generic_rows((unsigned*)GLB(unsigned, Qp) + ho, (unsigned*)GLB(unsigned, Kp) + ho, (unsigned*)GLB(unsigned, Vp) + ho, (unsigned*)GLB(unsigned, Op) + ho, r0 + 16 * w, 16, win, ng, lane);
        return;
    }
    if (w >= 12) WAITVM(8); else WAITVM(4);
    flag_set(fa_, 1);
    unsigned tag = LDS(unsigned, lb + AUXF + 128);
    f32x4 s[5], sg;
    unsigned kad[4];
    mask_init(s, r0 + 16 * w, c16, g, win, ng);
#pragma unroll
    for (int r = 0; r < 4; ++r) sg[r] = (4 * g + r < ng) ? 0.f : -__builtin_inff();
#pragma unroll
    for (int t = 0; t < 4; ++t) kad[t] = lb + KB + w * TILEB + c16 * ROWB + (((4 * t + g) ^ c16) << 4);
    asm volatile("" : "+v"(s[0]), "+v"(s[1]), "+v"(s[2]), "+v"(s[3]), "+v"(s[4]), "+v"(sg), "+v"(kad[0]), "+v"(kad[1]), "+v"(kad[2]), "+v"(kad[3]), "+v"(tag));
    tag = __builtin_amdgcn_readfirstlane(tag);
    flag_wait(fad, ((31u << w) >> 4) | 15u, 1);
#define MM4(acc, f) { acc = mfma(f[0], qf[0], acc); acc = mfma(f[1], qf[1], acc); acc = mfma(f[2], qf[2], acc); acc = mfma(f[3], qf[3], acc); }
#define KRD(f, i) { f[0] = LDS(u32x4, kad[0] + (i) * TILEB); f[1] = LDS(u32x4, kad[1] + (i) * TILEB); f[2] = LDS(u32x4, kad[2] + (i) * TILEB); f[3] = LDS(u32x4, kad[3] + (i) * TILEB); }
    u32x4 kb[4], kg[4], qf[4];
    const unsigned ga = lb + AUXQ + c16 * ROWB + ((g ^ c16) << 4);
    KRD(kb, 2)
    {
#pragma unroll
        for (int n = 0; n < 4; ++n) kg[n] = (u32x4){0u, 0u, 0u, 0u};
        if (c16 < 2) {
#pragma unroll
            for (int n = 0; n < 4; ++n) kg[n] = LDS(u32x4, ga + 64 * n);
        }
        f32x4 t = {0.f, 0.f, 0.f, 0.f};
        t = mfma(kb[0], kg[0], t); t = mfma(kb[1], kg[1], t); t = mfma(kb[2], kg[2], t); t = mfma(kb[3], kg[3], t);
        if (c16 < 2) LDS(f32x4, lb + AUXS + c16 * 1024 + w * 64 + g * 16) = t;
        flag_set(fa_, 2);
    }
    const int key = lane >> 2;
    const unsigned vad0 = key * ROWB + ((key & 7) << 5) + ((lane & 3) << 3);
    const u32x4 ones = {0x3F803F80u, 0x3F803F80u, 0x3F803F80u, 0x3F803F80u};
    const __amdgpu_buffer_rsrc_t ro = __builtin_amdgcn_make_buffer_rsrc((char*)GLB(char, Op), 0, NH * HEADB, 0x00020000);
    const __amdgpu_buffer_rsrc_t rw = __builtin_amdgcn_make_buffer_rsrc(ws + WS_REC + head * (NCH * 4096), 0, NCH * 4096, 0x00020000);
    const __amdgpu_buffer_rsrc_t rf = __builtin_amdgcn_make_buffer_rsrc(ws + WS_FLG, 0, 256 * 64, 0x00020000);
    if (w >= 8) {
        flag_wait(fad, 0xffffu, 2);
        const unsigned as = lb + AUXS + (c16 & 1) * 1024 + (c16 >> 1) * 128 + g * 16;
        const f32x4 x = LDS(f32x4, as), y = LDS(f32x4, as + 64);
        float m = max3(max3(x[0], x[1], x[2]), x[3], y[0]);
        m = xmax(fmaxf(max3(m, y[1], y[2]), y[3]));
        m = fmaxf(m, rotf<2>(m)); m = fmaxf(m, rotf<4>(m)); m = fmaxf(m, rotf<8>(m));
        const float mc = -m * C2;
        u32x4 pg = {expk(x[0], x[1], mc), expk(x[2], x[3], mc), expk(y[0], y[1], mc), expk(y[2], y[3], mc)};
        float l = mfma(ones, pg, (f32x4){0.f, 0.f, 0.f, 0.f})[0];
        l += rotf<2>(l); l += rotf<4>(l); l += rotf<8>(l);
        float rlg = __builtin_amdgcn_rcpf(l), lse = __builtin_amdgcn_logf(l) - mc;
        unsigned a0 = lb + VB + 2 * TILEB + (vad0 ^ ((w - 8) << 5));
        unsigned off = c16 < 2 ? c16 * 4096 + (chunk * 8 + (w & 1) * 4 + g) * 16 : OOR;
        asm volatile("" : "+v"(pg), "+v"(rlg), "+v"(lse), "+v"(a0), "+v"(off));
        WAITVM(0);
        flag_set(fb_, 2);
        flag_wait(fad, 0x00ff0000u, 1);
        __builtin_amdgcn_s_setprio(2);
        DMA4(lb + KB + (w - 8) * TILEB, vk0, v1, v2, v3, rq, qs);
        f32x4 acc = {0.f, 0.f, 0.f, 0.f};
#define GSTEP(n, p) acc = mfma(pair(trread(a0 + 2 * (n) * TILEB), trread(a0 + (2 * (n) + 1) * TILEB)), p, acc);
        flag_wait(fad, 0xff000000u, 2);
        GSTEP(0, pg) GSTEP(1, rot4<2>(pg)) GSTEP(2, rot4<4>(pg)) GSTEP(3, rot4<6>(pg)) GSTEP(4, rot4<8>(pg))
        u32x4 p5 = rot4<10>(pg), p6 = rot4<12>(pg), p7 = rot4<14>(pg);
        asm volatile("" : "+v"(acc), "+v"(p5), "+v"(p6), "+v"(p7));
        flag_wait(fad, 0x003f0000u, 2);
        GSTEP(5, p5) GSTEP(6, p6) GSTEP(7, p7)
        SGB(0x100, 6); SGB(0x8, 3);
#define REC(a, b, o) __builtin_amdgcn_raw_buffer_store_b128((u32x4){__float_as_uint(a), tag, __float_as_uint(b), tag}, rw, off + (o), (w & 6) * 4096, SC1)
        REC(lse, 0.f, 2048); REC(acc[0] * rlg, acc[1] * rlg, 0); REC(acc[2] * rlg, acc[3] * rlg, 1024);
        flag_set(fb_, 3);
        __builtin_amdgcn_s_setprio(1);
    }
    if (c16 < 2) {
#pragma unroll
        for (int n = 0; n < 4; ++n) kg[n] = LDS(u32x4, ga + (AUXK - AUXQ) + 64 * n);
    }
    if (w != 15) KRD(ka, 4)
    unsigned vad[8], vga = lb + AUXV + (vad0 & 0x13f), oa = lb + KB + w * TILEB + c16 * ROWB + (((g >> 1) ^ (c16 & 7)) << 4) + (((g << 3) ^ c16) & 8);
    {
        unsigned va = lb + VB + w * TILEB + vad0;
        asm volatile("" : "+v"(va));
#pragma unroll
        for (int b = 0; b < 8; ++b) vad[b] = va ^ (b << 5);
        asm volatile("" : "+v"(vad[0]), "+v"(vad[1]), "+v"(vad[2]), "+v"(vad[3]), "+v"(vad[4]), "+v"(vad[5]), "+v"(vad[6]), "+v"(vad[7]), "+v"(vga), "+v"(oa));
    }
    if (w >= 8) { flag_wait(fad, 0x00ff0000u, 2); WAITVM(3); } else WAITVM(0);
#pragma unroll
    for (int t = 0; t < 4; ++t) qf[t] = LDS(u32x4, kad[t] + (w < 8 ? VB - KB + 12 * TILEB : -8 * TILEB));
    SB();
#define NAP(x) { asm volatile("" : "+v"(x)); if (w < 8) __builtin_amdgcn_s_sleep(5); }
    MM4(s[2], kb) SB(); NAP(s[2])
    KRD(kb, 0) MM4(sg, kg) SB(); NAP(sg)
    KRD(kg, 1) MM4(s[4], ka) SB(); NAP(s[4])
    KRD(ka, 3) flag_set(fa_, 3);
    MM4(s[0], kb) SB(); NAP(s[0])
    MM4(s[1], kg) NAP(s[1]) MM4(s[3], ka)
    SB();
    u32x4 fa[8], f1[8], f2[8];
    u32x2 vg[8];
#define VREAD(b) { fa[b] = pair(vg[b], trread(vad[b])); f1[b] = pair(trread(vad[b] + TILEB), trread(vad[b] + 2 * TILEB)); \
        f2[b] = pair(trread(vad[b] + 3 * TILEB), trread(vad[b] + 4 * TILEB)); }
#pragma unroll
    for (int b = 0; b < 8; ++b) vg[b] = trread(vga ^ (b << 5));
    if (w < 8) {
        __builtin_amdgcn_s_setprio(2);
        VTILE(w + 12, (qs + 10 * TILEB) | (((chunk * 16 + w + 10) & 128) << 24));
        if (w == 6) __builtin_amdgcn_raw_buffer_store_b32(tag, rf, lane < 16 ? lane * 4 : OOR, blk * 64, SC1);
        flag_set(fb_, 1);
        __builtin_amdgcn_s_setprio(1);
    } else { VREAD(0) VREAD(1) VREAD(2) }
    u32x4 p0, p1, p2; float rl;
    {
        float m = max3(sg[0], sg[1], s[2][0]), m2 = max3(s[2][1], s[2][2], s[2][3]);
        m = max3(m, s[4][0], s[4][1]); m2 = max3(m2, s[4][2], s[4][3]);
        m = max3(m, s[0][0], s[0][1]); m2 = max3(m2, s[0][2], s[0][3]);
        m = max3(m, s[1][0], s[1][1]); m2 = max3(m2, s[1][2], s[1][3]);
        m = max3(m, s[3][0], s[3][1]); m2 = max3(m2, s[3][2], s[3][3]);
        const float mc = -xmax(fmaxf(m, m2)) * C2;
        p0 = (u32x4){expk(sg[0], sg[1], mc), 0u, expk(s[0][0], s[0][1], mc), expk(s[0][2], s[0][3], mc)};
        p1 = (u32x4){expk(s[1][0], s[1][1], mc), expk(s[1][2], s[1][3], mc), expk(s[2][0], s[2][1], mc), expk(s[2][2], s[2][3], mc)};
        p2 = (u32x4){expk(s[3][0], s[3][1], mc), expk(s[3][2], s[3][3], mc), expk(s[4][0], s[4][1], mc), expk(s[4][2], s[4][3], mc)};
        f32x4 l = {0.f, 0.f, 0.f, 0.f};
        l = mfma(ones, p0, l); l = mfma(ones, p1, l); l = mfma(ones, p2, l);
        rl = __builtin_amdgcn_rcpf(l[0]);
    }
    asm volatile("" : "+v"(p0), "+v"(p1), "+v"(p2), "+v"(rl));
    if (w < 8) {
        if (w >= 4) { if (w == 6) WAITVM(1); else WAITVM(0); flag_set(fb_, 2); }
        flag_wait(fad, 0xff000000u, 2);
        VREAD(0) VREAD(1) VREAD(2)
    }
    SB();
    u32x2 op[8]; unsigned fl;
    {
        f32x4 o[8];
#define PVMM(b) { o[b] = mfma(fa[b], p0, (f32x4){0.f, 0.f, 0.f, 0.f}); o[b] = mfma(f1[b], p1, o[b]); o[b] = mfma(f2[b], p2, o[b]); }
#define OWR(b) { op[b] = (u32x2){mulpk(o[b][0], o[b][1], rl), mulpk(o[b][2], o[b][3], rl)}; }
#define WEAVE() { SGB(0x8, 1); SGB(0x100, 2); SGB(0x8, 1); SGB(0x100, 2); SGB(0x8, 1); SGB(0x100, 1); SGB(0x2, 4); SB(); }
        VREAD(3) PVMM(0) WEAVE()
        VREAD(4) PVMM(1) OWR(0) WEAVE()
        VREAD(5) PVMM(2) OWR(1) WEAVE()
        VREAD(6) PVMM(3) OWR(2) WEAVE()
        VREAD(7) PVMM(4) OWR(3) WEAVE()
        PVMM(5) OWR(4) WEAVE()
        asm volatile("ds_read_b32 %0, %1" : "=v"(fl) : "v"(fad) : "memory");
        PVMM(6) OWR(5) WEAVE()
        PVMM(7) OWR(6) WEAVE()
        OWR(7)
    }
    if (w < 4) { WAITVM(0); flag_set(fb_, 2); }
    asm volatile("s_waitcnt lgkmcnt(0)" : "+v"(fl), "+v"(op[0]), "+v"(op[1]), "+v"(op[2]), "+v"(op[3]), "+v"(op[4]), "+v"(op[5]), "+v"(op[6]), "+v"(op[7]));
    if (w == 0) {
        flag_wait(fad, 0xff000000u, 3);
        u32x4 x = {lane * 64u, lane * 64u + 4096, lane * 64u + 8192, lane * 64u + 12288};
#pragma unroll 1
        for (unsigned spins = 0; spins < (1u << 20); ++spins) {
            unsigned a, b, c, d;
            asm volatile("buffer_load_dword %0, %4, %8, 0 offen sc1\n\tbuffer_load_dword %1, %5, %8, 0 offen sc1\n\t"
                         "buffer_load_dword %2, %6, %8, 0 offen sc1\n\tbuffer_load_dword %3, %7, %8, 0 offen sc1\n\ts_waitcnt vmcnt(0)"
                         : "=&v"(a), "=&v"(b), "=&v"(c), "=&v"(d) : "v"(x[0]), "v"(x[1]), "v"(x[2]), "v"(x[3]), "s"(rf) : "memory");
            x = (u32x4){a == tag ? OOR : x[0], b == tag ? OOR : x[1], c == tag ? OOR : x[2], d == tag ? OOR : x[3]};
            if (__all((int)(x[0] & x[1] & x[2] & x[3]) < 0)) { if (spins) __builtin_amdgcn_s_sleep(6); break; }
            __builtin_amdgcn_s_sleep(2);
        }
        flag_set(fa_, 4);
        if (blk == 0 && lane == 0) __hip_atomic_store((unsigned*)ws, tag, __ATOMIC_RELAXED, __HIP_MEMORY_SCOPE_AGENT);
    }
    {
        const unsigned need = w < 8 ? 256u << w : (31u << w) >> 4;
        if (((unsigned)__ballot(fl >= 3) & need) != need) flag_wait(fad, need, 3);
    }
#pragma unroll
    for (int b = 0; b < 8; ++b) LDS(u32x2, (oa ^ ((b & 3) << 5)) + (b >> 2) * 128) = op[b];
    asm volatile("" ::: "memory");
    u32x4 orow[4];
#pragma unroll
    for (int i = 0; i < 4; ++i) {
        const u32x4 x = LDS(u32x4, ((lb + KB + w * TILEB + vk0) ^ ((i & 1) << 6)) + i * 1024);
        orow[i] = i < 2 ? x : (u32x4){x[2], x[3], x[0], x[1]};
    }
#define OSTORE() { if (w != 0 && __builtin_amdgcn_readfirstlane(fl) < 4) flag_wait(fad, 1u, 4); \
        __builtin_amdgcn_raw_buffer_store_b128(orow[0], ro, (r0 + 16 * w + g < ng) ? OOR : lane * 16, qs, SC1);     \
        _Pragma("unroll") for (int i = 1; i < 4; ++i) __builtin_amdgcn_raw_buffer_store_b128(orow[i], ro, lane * 16 + i * 1024, qs, SC1); }
    bool first = false;
    if (w == 7) {
        __builtin_amdgcn_s_sleep(6);
        asm volatile("ds_read_b32 %0, %1\n\ts_waitcnt lgkmcnt(0)" : "=v"(fl) : "v"(fad) : "memory");
        first = __builtin_amdgcn_readfirstlane(fl) < 4;
    }
    if (!first) OSTORE()
    if (w == 7) {
        u32x4 x0, x1; u32x2 x2;
#pragma unroll 1
        for (unsigned spins = 0; spins < (1u << 20); ++spins) {
            asm volatile("buffer_load_dwordx4 %0, %3, %4, %5 offen sc1\n\tbuffer_load_dwordx4 %1, %3, %4, %5 offen offset:1024 sc1\n\t"
                         "buffer_load_dwordx2 %2, %3, %4, %5 offen offset:2048 sc1\n\ts_waitcnt vmcnt(0)"
                         : "=&v"(x0), "=&v"(x1), "=&v"(x2) : "v"((lane & 7) * 128 + (lane >> 3) * 16), "s"(rw), "s"(chunk * 4096) : "memory");
            if (__all(x0[1] == tag && x0[3] == tag && x1[1] == tag && x1[3] == tag && x2[1] == tag)) break;
        }
        const float ls = __uint_as_float(x2[0]);
        const float f = __builtin_amdgcn_exp2f(ls - max8(ls));
        const float r = __builtin_amdgcn_rcpf(sum8(f));
        const float a0 = sum8(f * __uint_as_float(x0[0])), a1 = sum8(f * __uint_as_float(x0[2]));
        const float a2 = sum8(f * __uint_as_float(x1[0])), a3 = sum8(f * __uint_as_float(x1[2]));
        const unsigned off = ((lane & 7) == 0 && (int)(chunk & 1) < ng) ? lane : OOR;
        __builtin_amdgcn_raw_buffer_store_b64((u32x2){mulpk(a0, a1, r), mulpk(a2, a3, r)}, ro, off, head * HEADB + (chunk & 1) * ROWB + (chunk >> 1) * 64, SC1);
        if (first) OSTORE()
    }
}

}

extern "C" void kernel_launch(void* const* d_in, const int* in_sizes, int n_in, void* d_out, int out_size, void* d_ws, size_t ws_size,
                              hipStream_t stream) {
    gl_attn<<<NH * NCH, 1024, 0, stream>>>(d_in[1], d_in[0], d_in[2], d_out, d_ws, (const int*)d_in[3], (const int*)d_in[4]);
}
```
